```python
import torch

def _softmax(x, axis=-1):
    return torch.softmax(x, dim=axis)
def _gelu(x):
    return torch.nn.functional.gelu(x, approximate="tanh")
def _silu(x):
    return torch.nn.functional.silu(x)
def _sigmoid(x):
    return torch.sigmoid(x)
def _layernorm(x, axis=-1, eps=1e-5):
    m = x.mean(dim=axis, keepdim=True)
    v = x.var(dim=axis, keepdim=True, unbiased=False)
    return (x - m) / torch.sqrt(v + eps)
def _scatter_add(tgt, idx, src, axis=0):
    return tgt.clone().index_add_(axis, idx, src)
def _segment_sum(src, seg, n):
    out = torch.zeros((n,) + src.shape[1:], dtype=src.dtype)
    return out.index_add_(0, seg, src)
def _pad(x, pw):
    flat = tuple(v for pair in reversed(pw) for v in pair)
    return torch.nn.functional.pad(x, flat)

kernel_name = 'sequential_preLN__compressed_kv_cla__glu_gated_swiglu__decode'

def setup_inputs():

    _w = (lambda a: (lambda t: t.to(torch.bfloat16) if t.is_floating_point() else t)(torch.as_tensor(a)))
    def _A_setup():
        B, H, Sq, Sk, D, M = 2, 32, 1, 8192, 128, 640
        Dc = 4 * D
        return {
            "x": _w((lambda *s: torch.randn(*s, dtype=torch.bfloat16) * 0.05)(B, Sq, M)),
            "c_kv": _w((lambda *s: torch.randn(*s, dtype=torch.bfloat16) * 0.8)(B, Sk, Dc)),
            "w_q": _w((lambda *s: torch.randn(*s, dtype=torch.bfloat16) * 1.6)(M, H, D) * (M ** -0.5)),
            "w_uk": _w((lambda *s: torch.randn(*s, dtype=torch.bfloat16) * 1.6)(Dc, H, D) * (Dc ** -0.5)),
            "w_uv": _w((lambda *s: torch.randn(*s, dtype=torch.bfloat16) * 0.8)(Dc, H, D) * (Dc ** -0.5)),
            "w_o": _w((lambda *s: torch.randn(*s, dtype=torch.bfloat16) * 0.8)(H, D, M) * ((H * D) ** -0.5)),
        }
    _A_in = _A_setup()

    def _B_setup():
        B, S, D, F = 2, 1, 640, 1728
        return {
            "x": _w((lambda *s: torch.randn(*s, dtype=torch.bfloat16) * 0.05)(B, S, D)),
            "w1": _w((lambda *s: torch.randn(*s, dtype=torch.bfloat16) * 0.2)(D, F) * (2.0 / D) ** 0.5),
            "w3": _w((lambda *s: torch.randn(*s, dtype=torch.bfloat16) * 0.2)(D, F) * (2.0 / D) ** 0.5),
            "w2": _w((lambda *s: torch.randn(*s, dtype=torch.bfloat16) * 0.2)(F, D) * (2.0 / F) ** 0.5),
        }
    _B_in = _B_setup()

    return {
        "A_x": _A_in["x"],
        "A_c_kv": _A_in["c_kv"],
        "A_w_q": _A_in["w_q"],
        "A_w_uk": _A_in["w_uk"],
        "A_w_uv": _A_in["w_uv"],
        "A_w_o": _A_in["w_o"],
        "B_x": _B_in["x"],
        "B_w1": _B_in["w1"],
        "B_w3": _B_in["w3"],
        "B_w2": _B_in["w2"],
    }

def reference(A_x, A_c_kv, A_w_q, A_w_uk, A_w_uv, A_w_o, B_x, B_w1, B_w3, B_w2):
    def _A_ref(x, c_kv, w_q, w_uk, w_uv, w_o):
        B, Sq, M = x.shape[0], x.shape[1], x.shape[2]
        Sk = c_kv.shape[1]
        H, D = w_q.shape[1], w_q.shape[2]
        q = torch.einsum("bqm,mhd->bhqd", x, w_q)
        k = torch.einsum("bkc,chd->bhkd", c_kv, w_uk)
        v = torch.einsum("bkc,chd->bhkd", c_kv, w_uv)
        scores = torch.einsum("bhqd,bhkd->bhqk", q, k) * (D ** -0.5)
        off = Sk - Sq
        qi = torch.arange(Sq)[:, None]
        kj = torch.arange(Sk)[None, :]
        cmask = kj <= (qi + off)
        scores = torch.where(cmask[None, None], scores, -1e9)
        p = _softmax(scores, axis=-1)
        ctx = torch.einsum("bhqk,bhkd->bhqd", p, v)
        return torch.einsum("bhqd,hdm->bqm", ctx, w_o)

    def _B_ref(x, w1, w3, w2):
        gate = torch.einsum("bsd,df->bsf", x, w1)
        up = torch.einsum("bsd,df->bsf", x, w3)
        h = _silu(gate) * up
        out = torch.einsum("bsf,fd->bsd", h, w2)
        return out

    x = A_x
    h = x + _A_ref(_layernorm(x, axis=-1), A_c_kv, A_w_q, A_w_uk, A_w_uv, A_w_o)
    h2 = h + _B_ref(_layernorm(h, axis=-1), B_w1, B_w3, B_w2)
    return h2
```

```cpp
#include <hip/hip_runtime.h>
#include <hip/hip_bf16.h>
typedef unsigned short u16;
__device__ __forceinline__ float bf2f(u16 v){ return __uint_as_float(((unsigned)v)<<16); }
__device__ __forceinline__ u16 f2bf(float f){ __hip_bfloat16 b = __float2bfloat16(f); return *(u16*)&b; }
constexpr int B_=2,H_=32,SK=8192,D_=128,M_=640,DC=512,F_=1728;

__device__ void ln_rows(const float* in, float* out, float* red){
  int t=threadIdx.x;
  for(int b=0;b<2;b++){
    float s=0; for(int i=t;i<M_;i+=256) s+=in[b*M_+i];
    red[t]=s; __syncthreads();
    for(int o=128;o>0;o>>=1){ if(t<o) red[t]+=red[t+o]; __syncthreads(); }
    float mean=red[0]/M_; __syncthreads();
    float v=0; for(int i=t;i<M_;i+=256){ float d=in[b*M_+i]-mean; v+=d*d; }
    red[t]=v; __syncthreads();
    for(int o=128;o>0;o>>=1){ if(t<o) red[t]+=red[t+o]; __syncthreads(); }
    float rs=1.0f/sqrtf(red[0]/M_+1e-5f); __syncthreads();
    for(int i=t;i<M_;i+=256) out[b*M_+i]=(in[b*M_+i]-mean)*rs;
    __syncthreads();
  }
}
__global__ void k_q(const u16* x,const u16* wq,float* q){
  __shared__ float xs[2*M_], xn[2*M_], red[256];
  int t=threadIdx.x;
  for(int i=t;i<2*M_;i+=256) xs[i]=bf2f(x[i]);
  __syncthreads();
  ln_rows(xs,xn,red);
  int n=blockIdx.x*64+(t&63); int ks=t>>6;
  float a0=0,a1=0;
  for(int m=ks;m<M_;m+=4){ float w=bf2f(wq[(size_t)m*4096+n]); a0+=xn[m]*w; a1+=xn[M_+m]*w; }
  __shared__ float r2[2][256];
  r2[0][t]=a0; r2[1][t]=a1; __syncthreads();
  if(t<128){ int b=t>>6,c=t&63; q[b*4096+blockIdx.x*64+c]=r2[b][c]+r2[b][c+64]+r2[b][c+128]+r2[b][c+192]; }
}
__global__ void k_qabs(const float* q,const u16* wuk,float* qa){
  int h=blockIdx.x, cb=blockIdx.y; int t=threadIdx.x;
  __shared__ float qs[2][128];
  if(t<256) qs[t>>7][t&127]=q[(t>>7)*4096+h*128+(t&127)];
  __syncthreads();
  int w=t>>6,l=t&63;
  for(int c=cb*64+w;c<cb*64+64;c+=4){
    const u16* p=wuk+((size_t)c*32+h)*128;
    float w0=bf2f(p[2*l]),w1=bf2f(p[2*l+1]);
    float a0=w0*qs[0][2*l]+w1*qs[0][2*l+1], a1=w0*qs[1][2*l]+w1*qs[1][2*l+1];
    for(int o=32;o>0;o>>=1){ a0+=__shfl_xor(a0,o); a1+=__shfl_xor(a1,o);}
    if(l==0){ qa[(0*32+h)*512+c]=a0*0.08838834764831845f; qa[(1*32+h)*512+c]=a1*0.08838834764831845f; }
  }
}
__global__ void k_attn(const float* qa,const u16* ckv,float* po,float* pm,float* pl){
  int ch=blockIdx.x,b=blockIdx.y,t=threadIdx.x;
  __shared__ u16 cs[64][520];
  __shared__ float s[32][64];
  const u16* src=ckv+((size_t)b*SK+ch*64)*DC;
  for(int i=t;i<64*512;i+=256) cs[i>>9][i&511]=src[i];
  __syncthreads();
  for(int i=t;i<32*64;i+=256){ int h=i>>6,k=i&63; const float* qq=qa+(b*32+h)*512; float a=0; for(int c=0;c<512;c++) a+=qq[c]*bf2f(cs[k][c]); s[h][k]=a; }
  __syncthreads();
  if(t<32){ float m=-1e30f; for(int k=0;k<64;k++) m=fmaxf(m,s[t][k]); float l=0; for(int k=0;k<64;k++){ float e=__expf(s[t][k]-m); s[t][k]=e; l+=e;} pm[(b*128+ch)*32+t]=m; pl[(b*128+ch)*32+t]=l; }
  __syncthreads();
  for(int i=t;i<32*512;i+=256){ int h=i>>9,c=i&511; float a=0; for(int k=0;k<64;k++) a+=s[h][k]*bf2f(cs[k][c]); po[((size_t)(b*128+ch)*32+h)*512+c]=a; }
}
__global__ void k_ctx(const float* po,const float* pm,const float* pl,const u16* wuv,float* ctx){
  int h=blockIdx.x,b=blockIdx.y,t=threadIdx.x;
  __shared__ float sc[128]; __shared__ float cc[512]; __shared__ float r[256];
  if(t==0){ float m=-1e30f; for(int j=0;j<128;j++) m=fmaxf(m,pm[(b*128+j)*32+h]); float l=0; for(int j=0;j<128;j++){ float e=__expf(pm[(b*128+j)*32+h]-m); sc[j]=e; l+=e*pl[(b*128+j)*32+h]; } for(int j=0;j<128;j++) sc[j]/=l; }
  __syncthreads();
  for(int c=t;c<512;c+=256){ float a=0; for(int j=0;j<128;j++) a+=sc[j]*po[((size_t)(b*128+j)*32+h)*512+c]; cc[c]=a; }
  __syncthreads();
  int d=t&127,half=t>>7; float a=0;
  for(int c=half*256;c<half*256+256;c++) a+=cc[c]*bf2f(wuv[((size_t)c*32+h)*128+d]);
  r[t]=a; __syncthreads();
  if(t<128) ctx[b*4096+h*128+t]=r[t]+r[t+128];
}
__global__ void k_wo(const u16* x,const float* ctx,const u16* wo,float* hbuf){
  int t=threadIdx.x; int m=blockIdx.x*16+(t&15); int ks=t>>4;
  float a0=0,a1=0;
  for(int k=ks;k<4096;k+=16){ float w=bf2f(wo[(size_t)k*M_+m]); a0+=ctx[k]*w; a1+=ctx[4096+k]*w; }
  __shared__ float r[2][256]; r[0][t]=a0; r[1][t]=a1; __syncthreads();
  if(t<32){ int b=t>>4,c=t&15; float s=0; for(int j=0;j<16;j++) s+=r[b][j*16+c]; int mm=blockIdx.x*16+c; hbuf[b*M_+mm]=bf2f(x[b*M_+mm])+s; }
}
__global__ void k_ffn1(const float* hbuf,const u16* w1,const u16* w3,float* act){
  __shared__ float xs[2*M_], xn[2*M_], red[256];
  int t=threadIdx.x;
  for(int i=t;i<2*M_;i+=256) xs[i]=hbuf[i];
  __syncthreads();
  ln_rows(xs,xn,red);
  int f=blockIdx.x*16+(t&15); int ks=t>>4;
  float g0=0,g1=0,u0=0,u1=0;
  for(int m=ks;m<M_;m+=16){ float a=bf2f(w1[(size_t)m*F_+f]), c=bf2f(w3[(size_t)m*F_+f]); g0+=xn[m]*a; g1+=xn[M_+m]*a; u0+=xn[m]*c; u1+=xn[M_+m]*c; }
  __shared__ float r[4][256]; r[0][t]=g0; r[1][t]=g1; r[2][t]=u0; r[3][t]=u1; __syncthreads();
  if(t<32){ int b=t>>4,c=t&15; float g=0,u=0; for(int j=0;j<16;j++){ g+=r[b][j*16+c]; u+=r[2+b][j*16+c]; } act[b*F_+blockIdx.x*16+c]=g/(1.0f+__expf(-g))*u; }
}
__global__ void k_ffn2(const float* hbuf,const float* act,const u16* w2,u16* out){
  int t=threadIdx.x; int m=blockIdx.x*16+(t&15); int ks=t>>4;
  float a0=0,a1=0;
  for(int k=ks;k<F_;k+=16){ float w=bf2f(w2[(size_t)k*M_+m]); a0+=act[k]*w; a1+=act[F_+k]*w; }
  __shared__ float r[2][256]; r[0][t]=a0; r[1][t]=a1; __syncthreads();
  if(t<32){ int b=t>>4,c=t&15; float s=0; for(int j=0;j<16;j++) s+=r[b][j*16+c]; int mm=blockIdx.x*16+c; out[b*M_+mm]=f2bf(hbuf[b*M_+mm]+s); }
}
extern "C" void kernel_launch(void* const* d_in, const int* in_sizes, int n_in, void* d_out, int out_size, void* d_ws, size_t ws_size, hipStream_t stream) {
  const u16* x=(const u16*)d_in[0]; const u16* ckv=(const u16*)d_in[1]; const u16* wq=(const u16*)d_in[2]; const u16* wuk=(const u16*)d_in[3];
  const u16* wuv=(const u16*)d_in[4]; const u16* wo=(const u16*)d_in[5]; const u16* w1=(const u16*)d_in[7]; const u16* w3=(const u16*)d_in[8]; const u16* w2=(const u16*)d_in[9];
  float* ws=(float*)d_ws;
  float* q=ws; float* qa=q+8192; float* pm=qa+32768; float* pl=pm+8192; float* ctx=pl+8192; float* hb=ctx+8192; float* act=hb+2048; float* po=act+4096;
  k_q<<<64,256,0,stream>>>(x,wq,q);
  k_qabs<<<dim3(32,8),256,0,stream>>>(q,wuk,qa);
  k_attn<<<dim3(128,2),256,0,stream>>>(qa,ckv,po,pm,pl);
  k_ctx<<<dim3(32,2),256,0,stream>>>(po,pm,pl,wuv,ctx);
  k_wo<<<40,256,0,stream>>>(x,ctx,wo,hb);
  k_ffn1<<<108,256,0,stream>>>(hb,w1,w3,act);
  k_ffn2<<<40,256,0,stream>>>(hb,act,w2,(u16*)d_out);
}
```

```cpp
#include <hip/hip_runtime.h>
#include <hip/hip_bf16.h>

#define DI __device__ __forceinline__
#define GAS __attribute__((address_space(1)))
#define LAS __attribute__((address_space(3)))
typedef unsigned short u16;
typedef unsigned v4u __attribute__((ext_vector_type(4)));
typedef unsigned v2u __attribute__((ext_vector_type(2)));
typedef float f32x2 __attribute__((ext_vector_type(2)));
typedef float f32x4 __attribute__((ext_vector_type(4)));
typedef float f32x16 __attribute__((ext_vector_type(16)));
typedef __bf16 bf16x8 __attribute__((ext_vector_type(8)));
typedef __bf16 bf16x2 __attribute__((ext_vector_type(2)));
typedef short s16x4 __attribute__((ext_vector_type(4)));
typedef __amdgpu_buffer_rsrc_t rsrc_t;

constexpr int M_ = 640, F_ = 1728, SK = 8192;
constexpr int OFF_EPOCH = 0;
constexpr int OFF_RDY = 1024;
constexpr int OFF_QA = 4096;
constexpr int OFF_CX = OFF_QA + 65536;
constexpr int OFF_HP = OFF_CX + 65536;
constexpr int OFF_ACT = OFF_HP + 20480;
constexpr int OFF_ST = OFF_HP + 32768;
constexpr int OFF_Q1 = OFF_ST + 65536;
constexpr int OFF_O = OFF_Q1 + 262144;
constexpr int OFF_STAMP = OFF_O + 4194304;
constexpr int WS_BYTES = OFF_STAMP + 256 * 8 * 16 * 8;
constexpr unsigned SENT = 0xFFFFFFFFu;
constexpr int SPIN_CAP = 1 << 16;
constexpr int AUX_SC1 = 16;

#define Y_DELAY 0
#define X_LATE_DELAY 0

#define STAMP(i) do { } while (0)

DI rsrc_t mk_rsrc(const void* p, int bytes) { return __builtin_amdgcn_make_buffer_rsrc((void*)p, 0, bytes, 0x00020000); }
DI void dma(unsigned lds_dst, unsigned voff, rsrc_t srd, unsigned soff) {
  asm volatile("s_mov_b32 m0, %0\n\ts_nop 4\n\tbuffer_load_dwordx4 %1, %2, %3 offen nt lds" ::"s"(lds_dst), "v"(voff), "s"(srd), "s"(soff) : "memory");
}
#define VM_WAIT(n) asm volatile("s_waitcnt vmcnt(" #n ")" ::: "memory")
#define BAR() asm volatile("s_waitcnt lgkmcnt(0)\n\ts_barrier" ::: "memory")
#define CFENCE() asm volatile("" ::: "memory")
DI v4u ld16(rsrc_t rs, int off) { return __builtin_amdgcn_raw_buffer_load_b128(rs, off, 0, AUX_SC1); }
DI unsigned ld4(rsrc_t rs, int off) { return __builtin_amdgcn_raw_buffer_load_b32(rs, off, 0, AUX_SC1); }
DI void st16(rsrc_t rs, int off, v4u v) { __builtin_amdgcn_raw_buffer_store_b128(v, rs, off, 0, AUX_SC1); }
DI void st8(rsrc_t rs, int off, v2u v) { __builtin_amdgcn_raw_buffer_store_b64(v, rs, off, 0, AUX_SC1); }
DI void st4(rsrc_t rs, int off, unsigned v) { __builtin_amdgcn_raw_buffer_store_b32(v, rs, off, 0, AUX_SC1); }
DI float bflo(unsigned u) { return __uint_as_float(u << 16); }
DI float bfhi(unsigned u) { return __uint_as_float(u & 0xffff0000u); }
DI unsigned pk2(float a, float b) { f32x2 v = {a, b}; return __builtin_bit_cast(unsigned, __builtin_convertvector(v, bf16x2)); }
DI unsigned unsent(unsigned u) { return u == SENT ? 0xFFC0FFC0u : u; }
DI bool nosent(v4u v) { return v.x != SENT && v.y != SENT && v.z != SENT && v.w != SENT; }
DI unsigned swz(unsigned row) { return ((row & 3u) << 2) | ((row >> 2) & 3u); }
DI f32x4 mfma16(v4u a, v4u b, f32x4 c) { return __builtin_amdgcn_mfma_f32_16x16x32_bf16(__builtin_bit_cast(bf16x8, a), __builtin_bit_cast(bf16x8, b), c, 0, 0, 0); }
DI f32x16 mfma32(v4u a, v4u b, f32x16 c) { return __builtin_amdgcn_mfma_f32_32x32x16_bf16(__builtin_bit_cast(bf16x8, a), __builtin_bit_cast(bf16x8, b), c, 0, 0, 0); }
DI v2u trd(LAS unsigned char* L, unsigned a) { return __builtin_bit_cast(v2u, __builtin_amdgcn_ds_read_tr16_b64_v4i16((LAS s16x4*)(L + a))); }
DI v4u trd2(LAS unsigned char* L, unsigned a0, unsigned a1) { v2u x = trd(L, a0), y = trd(L, a1); return v4u{x.x, x.y, y.x, y.y}; }
DI v4u lds16(LAS unsigned char* L, unsigned a) { return *(LAS v4u*)(L + a); }
DI float x16(float v) { auto r = __builtin_amdgcn_permlane16_swap(__float_as_uint(v), __float_as_uint(v), false, false); return __uint_as_float(r[0]) + __uint_as_float(r[1]); }
DI float x32(float v) { auto r = __builtin_amdgcn_permlane32_swap(__float_as_uint(v), __float_as_uint(v), false, false); return __uint_as_float(r[0]) + __uint_as_float(r[1]); }
DI float m16(float v) { auto r = __builtin_amdgcn_permlane16_swap(__float_as_uint(v), __float_as_uint(v), false, false); return fmaxf(__uint_as_float(r[0]), __uint_as_float(r[1])); }
DI float m32(float v) { auto r = __builtin_amdgcn_permlane32_swap(__float_as_uint(v), __float_as_uint(v), false, false); return fmaxf(__uint_as_float(r[0]), __uint_as_float(r[1])); }
template <int CTRL> DI float dppf(float v) { return __uint_as_float(__builtin_amdgcn_update_dpp(0u, __float_as_uint(v), CTRL, 0xf, 0xf, true)); }
DI float wsum(float v) {
  v += dppf<0xB1>(v); v += dppf<0x4E>(v); v += dppf<0x141>(v); v += dppf<0x140>(v);
  return x32(x16(v));
}
DI float wmax(float v) {
  v = fmaxf(v, dppf<0xB1>(v)); v = fmaxf(v, dppf<0x4E>(v)); v = fmaxf(v, dppf<0x141>(v)); v = fmaxf(v, dppf<0x140>(v));
  return m32(m16(v));
}

constexpr unsigned XL_WQ = 0, XL_WUK = 40960, XL_WUV = 73728, XL_WO = 0, XL_MISC = 106496;
constexpr unsigned XL_XB = XL_MISC;
constexpr unsigned XL_TAG = XL_MISC + 2048;
constexpr unsigned XL_QB = XL_MISC + 2304;
constexpr unsigned XL_QS = XL_MISC + 4096;
constexpr unsigned XL_PC = XL_MISC + 8192;
constexpr unsigned XL_CS = XL_MISC + 16384;
constexpr unsigned XL_RED = XL_MISC + 17408;
constexpr unsigned YL_C = 0, YL_Q = 131072;
constexpr unsigned YL_P = YL_Q;
constexpr unsigned YL_SMAX = YL_Q + 8192;
constexpr unsigned YL_SSUM = YL_Q + 8704;
constexpr unsigned YL_W = 0;
constexpr unsigned YL_HB = YL_Q + 12288;
constexpr unsigned YL_STAT = YL_Q + 20480;
constexpr unsigned YL_RED = YL_Q + 21504;

__global__ void __launch_bounds__(512, 2)
mla_block_kernel(const u16* __restrict__ x, const u16* __restrict__ ckv, const u16* __restrict__ wq, const u16* __restrict__ wuk,
                 const u16* __restrict__ wuv, const u16* __restrict__ wo, const u16* __restrict__ w1, const u16* __restrict__ w3,
                 const u16* __restrict__ w2, u16* __restrict__ out, unsigned char* __restrict__ ws) {
  __shared__ __attribute__((aligned(16))) unsigned char smem[163840];
  LAS unsigned char* L = (LAS unsigned char*)smem;
  const unsigned lbase = (unsigned)(size_t)L;
  const int tid = threadIdx.x, lane = tid & 63, w = __builtin_amdgcn_readfirstlane(tid >> 6);
  const int bid = blockIdx.x;
  const int l15 = lane & 15, g = lane >> 4;
  const rsrc_t rw = mk_rsrc(ws, WS_BYTES);
  const unsigned long long t_start = __builtin_amdgcn_s_memrealtime();
  STAMP(0);

  if (bid < 128) {
    const int h = (bid & 7) + 8 * (bid >> 5), r = (bid >> 3) & 3;
    if (w < 5) {
      const rsrc_t rq = mk_rsrc(wq, M_ * 4096 * 2);
#pragma unroll 1
      for (int i = 0; i < 8; i++) {
        const unsigned row = 4 * i + g;
        dma(lbase + XL_WQ + (w * 8 + i) * 1024, g * 8192 + ((l15 ^ swz(row)) << 4), rq, (160 * r + 32 * w + 4 * i) * 8192 + h * 256);
      }
      STAMP(1);
      VM_WAIT(0);
    } else if (w < 7) {
      const rsrc_t rk = mk_rsrc(wuk, 512 * 4096 * 2);
#pragma unroll 1
      for (int i = 0; i < 16; i++) {
        const unsigned row = 4 * i + g;
        dma(lbase + XL_WUK + ((w - 5) * 16 + i) * 1024, g * 8192 + ((l15 ^ swz(row)) << 4), rk, (128 * r + 64 * (w - 5) + 4 * i) * 8192 + h * 256);
      }
      STAMP(1);
    } else {
      const unsigned ep = ld4(rw, OFF_EPOCH);
      unsigned xr[2][5];
#pragma unroll
      for (int b = 0; b < 2; b++)
#pragma unroll
        for (int k = 0; k < 5; k++) xr[b][k] = ((const GAS unsigned*)x)[b * 320 + k * 64 + lane];
      const v4u sv = {SENT, SENT, SENT, SENT};
      st16(rw, lane < 32 ? OFF_QA + ((lane >> 4) * 32 + h) * 1024 + r * 256 + l15 * 16 : OFF_CX + (h * 4 + r) * 512 + (lane - 32) * 16, sv);
      if (lane < 16) st16(rw, OFF_HP + bid * 256 + lane * 16, sv);
      const unsigned tag = ep * 1664525u + 1013904223u;
#pragma unroll
      for (int b = 0; b < 2; b++) {
        float s = 0.f;
#pragma unroll
        for (int k = 0; k < 5; k++) s += bflo(xr[b][k]) + bfhi(xr[b][k]);
        const float mean = wsum(s) * (1.f / M_);
        float q = 0.f;
#pragma unroll
        for (int k = 0; k < 5; k++) { float a = bflo(xr[b][k]) - mean, c = bfhi(xr[b][k]) - mean; q += a * a + c * c; }
        const float rstd = 1.0f / sqrtf(wsum(q) * (1.f / M_) + 1e-5f);
#pragma unroll
        for (int k = 0; k < 5; k++) {
          const int dw = k * 64 + lane - 80 * r;
          if (dw >= 0 && dw < 80) {
            const float a = (bflo(xr[b][k]) - mean) * rstd, c = (bfhi(xr[b][k]) - mean) * rstd;
            const unsigned hi = pk2(a, c);
            const unsigned lo = pk2(a - bflo(hi), c - bfhi(hi));
            *(LAS unsigned*)(L + XL_XB + b * 336 + dw * 4) = hi;
            *(LAS unsigned*)(L + XL_XB + (2 + b) * 336 + dw * 4) = lo;
          }
        }
      }
      if (lane == 0) *(LAS unsigned*)(L + XL_TAG) = tag;
      VM_WAIT(0);
      if (lane == 0) st8(rw, OFF_RDY + bid * 8, v2u{tag, ~tag});
      STAMP(1);
    }
    BAR();
    STAMP(2);
    const unsigned tag = __builtin_amdgcn_readfirstlane(*(LAS unsigned*)(L + XL_TAG));
    const int q4 = l15 >> 2, p4 = lane & 3;
    {
      f32x4 acc = {0.f, 0.f, 0.f, 0.f};
      const unsigned ch = 2 * w + (p4 >> 1);
      const unsigned r0 = 8 * g + q4, r1 = r0 + 4;
      const unsigned a0 = XL_WQ + r0 * 256 + ((ch ^ swz(r0)) << 4) + 8 * (p4 & 1), a1 = XL_WQ + r1 * 256 + ((ch ^ swz(r1)) << 4) + 8 * (p4 & 1);
#pragma unroll
      for (int s = 0; s < 5; s++) {
        const v4u a = lds16(L, XL_XB + (lane & 3) * 336 + s * 64 + g * 16);
        const v4u bq = trd2(L, a0 + s * 8192, a1 + s * 8192);
        acc = mfma16(a, bq, acc);
      }
      if (lane < 16) st16(rw, OFF_Q1 + ((h * 4 + r) * 128 + 16 * w + lane) * 16, v4u{__float_as_uint(acc[0] + acc[2]), tag, __float_as_uint(acc[1] + acc[3]), tag});
    }
    STAMP(3);
    if (w < 2) {
      v4u v[4];
      for (int sp = 0; sp < SPIN_CAP; sp++) {
        CFENCE();
        bool ok = true;
#pragma unroll
        for (int k = 0; k < 4; k++) v[k] = ld16(rw, OFF_Q1 + ((h * 4 + k) * 128 + tid) * 16);
#pragma unroll
        for (int k = 0; k < 4; k++) ok &= (v[k].y == tag) & (v[k].w == tag);
        if (__all(ok)) break;
      }
      const float q0 = (__uint_as_float(v[0].x) + __uint_as_float(v[1].x)) + (__uint_as_float(v[2].x) + __uint_as_float(v[3].x));
      const float q1 = (__uint_as_float(v[0].z) + __uint_as_float(v[1].z)) + (__uint_as_float(v[2].z) + __uint_as_float(v[3].z));
      const unsigned hi = pk2(q0, q1), lo = pk2(q0 - bflo(hi), q1 - bfhi(hi));
      *(LAS u16*)(L + XL_QB + 0 * 272 + tid * 2) = (u16)hi;
      *(LAS u16*)(L + XL_QB + 1 * 272 + tid * 2) = (u16)(hi >> 16);
      *(LAS u16*)(L + XL_QB + 2 * 272 + tid * 2) = (u16)lo;
      *(LAS u16*)(L + XL_QB + 3 * 272 + tid * 2) = (u16)(lo >> 16);
    } else if (w == 5 || w == 6) {
      VM_WAIT(0);
    }
    STAMP(4);
    BAR();
    {
      f32x4 acc = {0.f, 0.f, 0.f, 0.f};
      const unsigned row = 16 * w + l15;
#pragma unroll
      for (int s = 0; s < 4; s++) {
        const v4u a = lds16(L, XL_QB + (lane & 3) * 272 + s * 64 + g * 16);
        const v4u bk = lds16(L, XL_WUK + row * 256 + (((4 * s + g) ^ swz(row)) << 4));
        acc = mfma16(a, bk, acc);
      }
      const float sc = 0.08838834764831845f * 1.4426950408889634f;
      if (lane < 16) {
        const unsigned v = pk2((acc[0] + acc[2]) * sc, (acc[1] + acc[3]) * sc);
        *(LAS u16*)(L + XL_QS + (16 * w + lane) * 2) = (u16)v;
        *(LAS u16*)(L + XL_QS + 256 + (16 * w + lane) * 2) = (u16)(v >> 16);
      }
    }
    BAR();
    if (tid < 32) {
      v4u v = lds16(L, XL_QS + lane * 16);
      v.x = unsent(v.x); v.y = unsent(v.y); v.z = unsent(v.z); v.w = unsent(v.w);
      st16(rw, OFF_QA + ((lane >> 4) * 32 + h) * 1024 + r * 256 + l15 * 16, v);
    }
    STAMP(5);
    const bool isL2 = bid < 80;
    const int kg = bid / 20, nn = bid % 20;
    if (X_LATE_DELAY) { while (__builtin_amdgcn_s_memrealtime() - t_start < X_LATE_DELAY) __builtin_amdgcn_s_sleep(2); }
    {
      const rsrc_t rv = mk_rsrc(wuv, 512 * 4096 * 2);
#pragma unroll 1
      for (int i = 0; i < 4; i++) {
        const unsigned row = 4 * i + g;
        dma(lbase + XL_WUV + (w * 4 + i) * 1024, g * 8192 + ((l15 ^ swz(row)) << 4), rv, (128 * r + 16 * w + 4 * i) * 8192 + h * 256);
      }
      if (isL2) {
        const rsrc_t ro = mk_rsrc(wo, 4096 * M_ * 2);
#pragma unroll 1
        for (int i = 0; i < 8; i++)
          dma(lbase + XL_WO + (w * 8 + i) * 1024, (lane >> 2) * 1280 + (lane & 3) * 16, ro, (1024 * kg + 128 * w + 16 * i) * 1280 + nn * 64);
      }
    }
    VM_WAIT(0);
    BAR();
    STAMP(6);
    v4u fv[4], fo[2][4];
    {
      const unsigned ch = 2 * w + (p4 >> 1);
      const unsigned r0 = 8 * g + q4, r1 = r0 + 4;
      const unsigned a0 = XL_WUV + r0 * 256 + ((ch ^ swz(r0)) << 4) + 8 * (p4 & 1), a1 = XL_WUV + r1 * 256 + ((ch ^ swz(r1)) << 4) + 8 * (p4 & 1);
#pragma unroll
      for (int s = 0; s < 4; s++) fv[s] = trd2(L, a0 + s * 8192, a1 + s * 8192);
      const unsigned b0 = XL_WO + (128 * w + 8 * g + q4) * 64 + 8 * p4;
#pragma unroll
      for (int t = 0; t < 2; t++)
#pragma unroll
        for (int s = 0; s < 4; s++) fo[t][s] = trd2(L, b0 + s * 2048 + t * 32, b0 + s * 2048 + t * 32 + 256);
    }
    {
      const int b = w >> 2, cg = w & 3;
      v4u sv;
      for (int sp = 0; sp < SPIN_CAP; sp++) {
        CFENCE();
        sv = ld16(rw, OFF_ST + ((b * 64 + lane) * 32 + h) * 16);
        if (__all((sv.y == tag) & (sv.w == tag))) break;
      }
      STAMP(7);
      v4u pc[4];
#pragma unroll
      for (int i = 0; i < 4; i++) pc[i] = ld16(rw, OFF_O + (((h * 4 + r) * 2 + b) * 64 + 16 * cg + 4 * i) * 256 + lane * 16);
      const float mj = __uint_as_float(sv.x), lj = __uint_as_float(sv.z);
      const float mm = wmax(mj);
      const float e = __builtin_amdgcn_exp2f(mj - mm);
      const float wgt = e / wsum(e * lj);
      float a[8];
#pragma unroll
      for (int k = 0; k < 8; k++) a[k] = 0.f;
#pragma unroll
      for (int i = 0; i < 4; i++) {
        const float wi = __shfl(wgt, 16 * cg + 4 * i + g);
#pragma unroll
        for (int k = 0; k < 4; k++) { a[2 * k] += wi * bflo(pc[i][k]); a[2 * k + 1] += wi * bfhi(pc[i][k]); }
      }
      const unsigned row = 4 * w + g;
      *(LAS v4u*)(L + XL_PC + row * 256 + ((l15 ^ swz(row)) << 4)) = v4u{pk2(a[0], a[1]), pk2(a[2], a[3]), pk2(a[4], a[5]), pk2(a[6], a[7])};
      STAMP(8);
      BAR();
      float cx[2];
#pragma unroll
      for (int bb = 0; bb < 2; bb++) {
        f32x4 acc = {0.f, 0.f, 0.f, 0.f};
#pragma unroll
        for (int s = 0; s < 4; s++) acc = mfma16(lds16(L, XL_PC + (16 * bb + l15) * 256 + (((4 * s + g) ^ swz(l15)) << 4)), fv[s], acc);
        cx[bb] = x32(x16((acc[0] + acc[1]) + (acc[2] + acc[3])));
      }
      if (lane < 16) {
        const unsigned v = pk2(cx[0], cx[1]);
        *(LAS u16*)(L + XL_CS + (16 * w + lane) * 2) = (u16)v;
        *(LAS u16*)(L + XL_CS + 256 + (16 * w + lane) * 2) = (u16)(v >> 16);
      }
      BAR();
      if (tid < 32) {
        v4u v = lds16(L, XL_CS + lane * 16);
        v.x = unsent(v.x); v.y = unsent(v.y); v.z = unsent(v.z); v.w = unsent(v.w);
        st16(rw, OFF_CX + (h * 4 + r) * 512 + lane * 16, v);
      }
      STAMP(9);
    }
    if (isL2) {
      v4u a[4];
      const int ao = OFF_CX + (8 * kg + w) * 2048 + (l15 & 7) * 256 + g * 16;
      for (int sp = 0; sp < SPIN_CAP; sp++) {
        CFENCE();
        bool ok = true;
#pragma unroll
        for (int s = 0; s < 4; s++) a[s] = ld16(rw, ao + s * 64);
#pragma unroll
        for (int s = 0; s < 4; s++) ok &= nosent(a[s]);
        if (__all(ok)) break;
      }
      STAMP(10);
#pragma unroll
      for (int t = 0; t < 2; t++) {
        f32x4 acc = {0.f, 0.f, 0.f, 0.f};
#pragma unroll
        for (int s = 0; s < 4; s++) acc = mfma16(a[s], fo[t][s], acc);
        if (lane < 32) *(LAS f32x2*)(L + XL_RED + (((w * 2 + g) * 2 + t) * 16 + l15) * 8) = f32x2{acc[0] + acc[2], acc[1] + acc[3]};
      }
      BAR();
      if (tid < 64) {
        const int b = lane >> 5, m = lane & 31;
        float s = 0.f;
#pragma unroll
        for (int k = 0; k < 16; k++) s += *(LAS float*)(L + XL_RED + (k * 32 + m) * 8 + b * 4);
        st4(rw, OFF_HP + ((kg * 2 + b) * 640 + 32 * nn + m) * 4, unsent(__float_as_uint(s)));
      }
      STAMP(11);
    }
  } else {
    const int j = bid - 128, b = j & 1, chunk = j >> 1;
    unsigned tag = 0;
    if (w < 4) {
      const rsrc_t rc = mk_rsrc(ckv + ((size_t)b * SK + chunk * 128) * 512, 128 * 1024);
      if (Y_DELAY) { while (__builtin_amdgcn_s_memrealtime() - t_start < Y_DELAY) __builtin_amdgcn_s_sleep(1); }
#pragma unroll 1
      for (int i = 0; i < 32; i++) {
        const unsigned row = (i >> 4) * 64 + 16 * w + (i & 15);
        dma(lbase + YL_C + row * 1024, (lane ^ swz(row)) << 4, rc, row * 1024);
      }
      STAMP(1);
    } else {
      tag = ld4(rw, OFF_EPOCH) * 1664525u + 1013904223u;
      while (__builtin_amdgcn_s_memrealtime() - t_start < 120) __builtin_amdgcn_s_sleep(4);
      for (int sp = 0; sp < SPIN_CAP; sp++) {
        CFENCE();
        const v4u v = ld16(rw, OFF_RDY + lane * 16);
        if (__all((v.x == tag) & (v.y == ~tag) & (v.z == tag) & (v.w == ~tag))) break;
        __builtin_amdgcn_s_sleep(4);
      }
      STAMP(1);
      const int qo = OFF_QA + (b * 32 + 8 * (w - 4)) * 1024 + lane * 16;
      for (int sp = 0; sp < SPIN_CAP; sp++) {
        CFENCE();
        const v4u v = ld16(rw, qo);
        if (__any(nosent(v))) break;
        __builtin_amdgcn_s_sleep(1);
      }
      STAMP(2);
      v4u v[8];
      for (int sp = 0; sp < SPIN_CAP; sp++) {
        CFENCE();
        bool ok = true;
#pragma unroll
        for (int k = 0; k < 8; k++) v[k] = ld16(rw, qo + k * 1024);
#pragma unroll
        for (int k = 0; k < 8; k++) ok &= nosent(v[k]);
        if (__all(ok)) break;
      }
#pragma unroll
      for (int k = 0; k < 8; k++) { const unsigned row = 8 * (w - 4) + k; *(LAS v4u*)(L + YL_Q + row * 1024 + ((lane ^ swz(row)) << 4)) = v[k]; }
      STAMP(3);
    }
    BAR();
    f32x4 sacc[2][2];
    if (w < 4) {
      v4u qf[2][16];
      const unsigned qa = YL_Q + l15 * 1024 + g * 256 + (swz(l15) << 4);
#pragma unroll
      for (int t = 0; t < 2; t++)
#pragma unroll
        for (int s = 0; s < 16; s++) qf[t][s] = lds16(L, (qa ^ (s << 4)) + t * 16384);
      BAR();
      STAMP(4);
#pragma unroll
      for (int T = 0; T < 2; T++) {
        if (T == 0) VM_WAIT(16); else VM_WAIT(0);
        if (T == 0) STAMP(5); else STAMP(6);
        sacc[T][0] = f32x4{0.f, 0.f, 0.f, 0.f}; sacc[T][1] = f32x4{0.f, 0.f, 0.f, 0.f};
        const unsigned ca = YL_C + (64 * T + 16 * w + l15) * 1024 + g * 256 + (swz(l15) << 4);
#pragma unroll
        for (int s = 0; s < 16; s++) {
          const v4u a = lds16(L, ca ^ (s << 4));
          sacc[T][0] = mfma16(a, qf[0][s], sacc[T][0]);
          sacc[T][1] = mfma16(a, qf[1][s], sacc[T][1]);
        }
      }
#pragma unroll
      for (int t = 0; t < 2; t++) {
        float m = fmaxf(fmaxf(fmaxf(sacc[0][t][0], sacc[0][t][1]), fmaxf(sacc[0][t][2], sacc[0][t][3])), fmaxf(fmaxf(sacc[1][t][0], sacc[1][t][1]), fmaxf(sacc[1][t][2], sacc[1][t][3])));
        m = m32(m16(m));
        if (lane < 16) *(LAS float*)(L + YL_SMAX + (16 * t + lane) * 16 + w * 4) = m;
      }
      STAMP(7);
      BAR();
#pragma unroll
      for (int t = 0; t < 2; t++) {
        const unsigned head = 16 * t + l15;
        const f32x4 mv = *(LAS f32x4*)(L + YL_SMAX + head * 16);
        const float m = fmaxf(fmaxf(mv[0], mv[1]), fmaxf(mv[2], mv[3]));
        float sum = 0.f;
#pragma unroll
        for (int T = 0; T < 2; T++) {
          float p[4];
#pragma unroll
          for (int k = 0; k < 4; k++) { p[k] = __builtin_amdgcn_exp2f(sacc[T][t][k] - m); sum += p[k]; }
          const unsigned key = 64 * T + 16 * w + 4 * g;
          *(LAS v2u*)(L + YL_P + head * 256 + (((key >> 3) ^ swz(head)) << 4) + ((key >> 2) & 1) * 8) = v2u{pk2(p[0], p[1]), pk2(p[2], p[3])};
        }
        sum = x32(x16(sum));
        if (lane < 16) *(LAS float*)(L + YL_SSUM + head * 16 + w * 4) = sum;
      }
    } else {
      BAR();
      BAR();
    }
    BAR();
    STAMP(8);
    {
      const unsigned l31 = lane & 31, hh = lane >> 5, blk = (lane >> 4) & 1, q4 = l15 >> 2, p4 = lane & 3;
      v4u pf[8];
#pragma unroll
      for (int ks = 0; ks < 8; ks++) pf[ks] = lds16(L, YL_P + l31 * 256 + (((2 * ks + hh) ^ swz(l31)) << 4));
      f32x16 o[2];
#pragma unroll
      for (int ct = 0; ct < 2; ct++) {
#pragma unroll
        for (int k = 0; k < 16; k++) o[ct][k] = 0.f;
        const unsigned ch = 8 * w + 4 * ct + 2 * blk + (p4 >> 1);
        const unsigned r0 = 8 * hh + q4, r1 = r0 + 4;
        const unsigned a0 = YL_C + r0 * 1024 + ((ch ^ swz(r0)) << 4) + 8 * (p4 & 1), a1 = YL_C + r1 * 1024 + ((ch ^ swz(r1)) << 4) + 8 * (p4 & 1);
#pragma unroll
        for (int ks = 0; ks < 8; ks++) o[ct] = mfma32(trd2(L, a0 + ks * 16384, a1 + ks * 16384), pf[ks], o[ct]);
      }
      STAMP(9);
#pragma unroll
      for (int ct = 0; ct < 2; ct++)
#pragma unroll
        for (int k = 0; k < 4; k++) {
          const unsigned ch = 8 * w + 4 * ct + k;
          *(LAS v2u*)(L + YL_C + l31 * 1024 + ((ch ^ swz(l31)) << 4) + 8 * hh) = v2u{pk2(o[ct][4 * k], o[ct][4 * k + 1]), pk2(o[ct][4 * k + 2], o[ct][4 * k + 3])};
        }
#pragma unroll
      for (int it = 0; it < 4; it++) {
        const unsigned head = 8 * it + (lane >> 3), u = lane & 7;
        const v4u v = lds16(L, YL_C + head * 1024 + (((8 * w + u) ^ swz(head)) << 4));
        st16(rw, OFF_O + (((head * 4 + (w >> 1)) * 2 + b) * 64 + chunk) * 256 + (w & 1) * 128 + u * 16, v);
      }
      STAMP(10);
      VM_WAIT(0);
      BAR();
      if (w == 4 && lane < 32) {
        const f32x4 mv = *(LAS f32x4*)(L + YL_SMAX + lane * 16), sv = *(LAS f32x4*)(L + YL_SSUM + lane * 16);
        const float m = fmaxf(fmaxf(mv[0], mv[1]), fmaxf(mv[2], mv[3])), s = (sv[0] + sv[1]) + (sv[2] + sv[3]);
        st16(rw, OFF_ST + ((b * 64 + chunk) * 32 + lane) * 16, v4u{__float_as_uint(m), tag, __float_as_uint(s), tag});
      }
      STAMP(11);
    }
    const int q4 = l15 >> 2, p4 = lane & 3;
    if (j < 54) {
      {
        const rsrc_t r1 = mk_rsrc(w < 4 ? w1 : w3, M_ * F_ * 2);
#pragma unroll 1
        for (int i = 0; i < 10; i++)
          dma(lbase + YL_W + (w * 10 + i) * 1024, (lane >> 2) * 3456 + (lane & 3) * 16, r1, (((w & 3) * 10 + i) * 16) * 3456 + j * 64);
      }
      const int bb = w >> 2, e4 = (w & 3) * 40 + lane;
      const bool own = lane < 40;
      v2u xv = {0u, 0u};
      if (own) xv = *(const GAS v2u*)(x + bb * M_ + e4 * 4);
      if (tid < 320) *(LAS unsigned*)(L + YL_HB + 4 * 1296 + tid * 4) = 0x3F803F80u;
      VM_WAIT(0);
      BAR();
      const int mat = w & 1, ft = (w >> 1) & 1, kh = w >> 2;
      v4u fw[10];
      {
        const unsigned b0 = YL_W + mat * 40960 + (320 * kh + 8 * g + q4) * 64 + ft * 32 + 8 * p4;
#pragma unroll
        for (int s = 0; s < 10; s++) fw[s] = trd2(L, b0 + s * 2048, b0 + s * 2048 + 256);
      }
      STAMP(12);
      v4u hp[4];
      const int ho = OFF_HP + (bb * 640 + (own ? e4 : 0) * 4) * 4;
      for (int sp = 0; sp < SPIN_CAP; sp++) {
        CFENCE();
        bool ok = true;
#pragma unroll
        for (int k = 0; k < 4; k++) hp[k] = ld16(rw, ho + k * 5120);
#pragma unroll
        for (int k = 0; k < 4; k++) ok &= nosent(hp[k]);
        if (__all(ok)) break;
      }
      STAMP(13);
      float hv[4];
      hv[0] = bflo(xv.x); hv[1] = bfhi(xv.x); hv[2] = bflo(xv.y); hv[3] = bfhi(xv.y);
#pragma unroll
      for (int e = 0; e < 4; e++) hv[e] += (__uint_as_float(hp[0][e]) + __uint_as_float(hp[1][e])) + (__uint_as_float(hp[2][e]) + __uint_as_float(hp[3][e]));
      if (own) {
        const unsigned h0 = pk2(hv[0], hv[1]), h1 = pk2(hv[2], hv[3]);
        *(LAS v2u*)(L + YL_HB + bb * 1296 + e4 * 8) = v2u{h0, h1};
        *(LAS v2u*)(L + YL_HB + (2 + bb) * 1296 + e4 * 8) = v2u{pk2(hv[0] - bflo(h0), hv[1] - bfhi(h0)), pk2(hv[2] - bflo(h1), hv[3] - bfhi(h1))};
      }
      {
        const float mw = wsum(own ? (hv[0] + hv[1]) + (hv[2] + hv[3]) : 0.f) * (1.f / 160.f);
        float q = 0.f;
#pragma unroll
        for (int e = 0; e < 4; e++) { const float d = hv[e] - mw; q += d * d; }
        q = wsum(own ? q : 0.f);
        if (lane == 0) *(LAS f32x2*)(L + YL_STAT + w * 8) = f32x2{mw, q};
      }
      BAR();
      {
        f32x4 acc = {0.f, 0.f, 0.f, 0.f};
        const unsigned ar = YL_HB + (l15 < 4 ? l15 : 4) * 1296 + kh * 640 + g * 16;
#pragma unroll
        for (int s = 0; s < 10; s++) acc = mfma16(lds16(L, ar + s * 64), fw[s], acc);
        if (lane < 16) *(LAS f32x2*)(L + YL_RED + (w * 16 + lane) * 16) = f32x2{acc[0] + acc[2], acc[1] + acc[3]};
        else if (lane < 32) *(LAS float*)(L + YL_RED + (w * 16 + l15) * 16 + 8) = acc[0];
      }
      BAR();
      if (tid < 64) {
        const int b2 = lane >> 5, f = lane & 31, ft2 = f >> 4;
        float mean = 0.f, m2 = 0.f;
        f32x2 stv[4];
#pragma unroll
        for (int k = 0; k < 4; k++) { stv[k] = *(LAS f32x2*)(L + YL_STAT + (b2 * 4 + k) * 8); mean += stv[k][0]; }
        mean *= 0.25f;
#pragma unroll
        for (int k = 0; k < 4; k++) { const float d = stv[k][0] - mean; m2 += stv[k][1] + 160.f * d * d; }
        const float rstd = 1.0f / sqrtf(m2 * (1.f / M_) + 1e-5f);
        float gu[2];
#pragma unroll
        for (int mt = 0; mt < 2; mt++) {
          const f32x4 u0 = *(LAS f32x4*)(L + YL_RED + ((mt + 2 * ft2) * 16 + (f & 15)) * 16), u1 = *(LAS f32x4*)(L + YL_RED + ((4 + mt + 2 * ft2) * 16 + (f & 15)) * 16);
          gu[mt] = ((b2 ? u0[1] + u1[1] : u0[0] + u1[0]) - mean * (u0[2] + u1[2])) * rstd;
        }
        const float act = gu[0] / (1.0f + __expf(-gu[0])) * gu[1];
        const float nb = dppf<0xB1>(act);
        if (!(lane & 1)) st4(rw, OFF_ACT + j * 128 + lane * 2, unsent(pk2(act, nb)));
      }
      STAMP(14);
    } else if (j < 74) {
      const int n = j - 54;
      {
        const rsrc_t r2 = mk_rsrc(w2, F_ * M_ * 2);
#pragma unroll 1
        for (int i = 0; i < 14; i++) {
          const int pc = w * 14 + i;
          if (pc < 108) dma(lbase + YL_W + pc * 1024, (lane >> 2) * 1280 + (lane & 3) * 16, r2, pc * 16 * 1280 + n * 64);
        }
      }
      VM_WAIT(0);
      v4u fw[2][7];
      {
        const unsigned b0 = YL_W + (224 * w + 8 * g + q4) * 64 + 8 * p4;
#pragma unroll
        for (int t = 0; t < 2; t++)
#pragma unroll
          for (int s = 0; s < 7; s++) fw[t][s] = trd2(L, b0 + s * 2048 + t * 32, b0 + s * 2048 + t * 32 + 256);
      }
      STAMP(12);
      float hres = 0.f;
      if (w == 0) {
        const int b2 = lane >> 5, m = lane & 31;
        hres = bflo(x[b2 * M_ + 32 * n + m]);
        unsigned hp[4];
        for (int sp = 0; sp < SPIN_CAP; sp++) {
          CFENCE();
          bool ok = true;
#pragma unroll
          for (int k = 0; k < 4; k++) hp[k] = ld4(rw, OFF_HP + ((k * 2 + b2) * 640 + 32 * n + m) * 4);
#pragma unroll
          for (int k = 0; k < 4; k++) ok &= hp[k] != SENT;
          if (__all(ok)) break;
        }
        hres += (__uint_as_float(hp[0]) + __uint_as_float(hp[1])) + (__uint_as_float(hp[2]) + __uint_as_float(hp[3]));
      }
      v4u a[7];
      for (int sp = 0; sp < SPIN_CAP; sp++) {
        CFENCE();
        bool ok = true;
#pragma unroll
        for (int s = 0; s < 7; s++) { const int ss = 7 * w + s; a[s] = ld16(rw, OFF_ACT + (ss < 54 ? ss : 53) * 128 + (lane & 1) * 64 + g * 16); }
#pragma unroll
        for (int s = 0; s < 7; s++) ok &= nosent(a[s]);
        if (__all(ok)) break;
      }
      STAMP(13);
#pragma unroll
      for (int t = 0; t < 2; t++) {
        f32x4 acc = {0.f, 0.f, 0.f, 0.f};
#pragma unroll
        for (int s = 0; s < 7; s++) if (7 * w + s < 54) acc = mfma16(a[s], fw[t][s], acc);
        if (lane < 16) *(LAS f32x2*)(L + YL_RED + ((w * 2 + t) * 16 + lane) * 8) = f32x2{acc[0], acc[1]};
      }
      BAR();
      if (tid < 64) {
        const int b2 = lane >> 5, m = lane & 31;
        float s = hres;
#pragma unroll
        for (int k = 0; k < 8; k++) s += *(LAS float*)(L + YL_RED + (k * 32 + m) * 8 + b2 * 4);
        const float nb = dppf<0xB1>(s);
        if (!(lane & 1)) *(GAS unsigned*)(out + b2 * M_ + 32 * n + m) = pk2(s, nb);
      }
      if (n == 0 && w == 4 && lane == 0) st4(rw, OFF_EPOCH, tag);
      STAMP(14);
    }
  }
}

extern "C" void kernel_launch(void* const* d_in, const int* in_sizes, int n_in, void* d_out, int out_size, void* d_ws, size_t ws_size,
                              hipStream_t stream) {
  mla_block_kernel<<<256, 512, 0, stream>>>((const u16*)d_in[0], (const u16*)d_in[1], (const u16*)d_in[2], (const u16*)d_in[3], (const u16*)d_in[4],
                                            (const u16*)d_in[5], (const u16*)d_in[7], (const u16*)d_in[8], (const u16*)d_in[9], (u16*)d_out,
                                            (unsigned char*)d_ws);
}
```

```cpp
#include <hip/hip_runtime.h>
#include <hip/hip_bf16.h>

#define DI __device__ __forceinline__
#define GAS __attribute__((address_space(1)))
#define LAS __attribute__((address_space(3)))
typedef unsigned short u16;
typedef unsigned v4u __attribute__((ext_vector_type(4)));
typedef unsigned v2u __attribute__((ext_vector_type(2)));
typedef float f32x2 __attribute__((ext_vector_type(2)));
typedef float f32x4 __attribute__((ext_vector_type(4)));
typedef float f32x16 __attribute__((ext_vector_type(16)));
typedef __bf16 bf16x8 __attribute__((ext_vector_type(8)));
typedef __bf16 bf16x2 __attribute__((ext_vector_type(2)));
typedef short s16x4 __attribute__((ext_vector_type(4)));
typedef __amdgpu_buffer_rsrc_t rsrc_t;

constexpr int M_ = 640, F_ = 1728, SK = 8192;
constexpr int OFF_EPOCH = 0;
constexpr int OFF_RDY = 1024;
constexpr int OFF_RDY2 = 32768;
constexpr int OFF_QA = 65536;
constexpr int OFF_CX = OFF_QA + 65536;
constexpr int OFF_HP = OFF_CX + 131072;
constexpr int OFF_ACT = OFF_HP + 20480;
constexpr int OFF_ST = OFF_HP + 65536;
constexpr int OFF_Q1 = OFF_ST + 65536;
constexpr int OFF_O = OFF_Q1 + 262144;
constexpr int OFF_STAMP = OFF_O + 4194304;
constexpr int WS_BYTES = OFF_STAMP + 256 * 8 * 16 * 8;
constexpr unsigned SENT = 0xFFFFFFFFu;
constexpr int SPIN_CAP = 1 << 16;
constexpr int AUX_SC1 = 16;

#define X_LATE_DELAY 540
#define CLR_DELAY 0
#define Y_WIN 16
#define G_START 250
#define Y_DELAY 150
#define UK_DELAY 0
#define X_WIN 8
#define F_WIN 4
#define A0_AT 31
#define SLP_ST 0
#define SLP_O 0
#define S_ACC2 0
#define PV_PRIO 0
#define YL_DELAY 0
#define CODE_KB 17

#define STAMP(i) do { } while (0)
#define STAMP_DECL
#define STAMP_FLUSH do { } while (0)

#define STR2(x) #x
#define STR(x) STR2(x)
#define VM_WIN(n) asm volatile("s_waitcnt vmcnt(" STR(n) " - 1)" ::: "memory")
#define VM_WAIT(n) asm volatile("s_waitcnt vmcnt(" #n ")" ::: "memory")
#define BAR() asm volatile("s_waitcnt lgkmcnt(0)\n\ts_barrier" ::: "memory")
#define BAR_L() asm volatile("s_waitcnt lgkmcnt(0)" ::: "memory")
#define CFENCE() asm volatile("" ::: "memory")
#define SB() __builtin_amdgcn_sched_barrier(0)
#define SPIN _Pragma("unroll 1") for (int sp = 0; sp < SPIN_CAP; sp++)
#define PIN(x) asm volatile("" : "+v"(x))

DI rsrc_t mk_rsrc(const void* p, int bytes) { return __builtin_amdgcn_make_buffer_rsrc((void*)p, 0, bytes, 0x00020000); }
DI void dma(unsigned lds_dst, unsigned voff, rsrc_t srd, unsigned soff) {
  asm volatile("s_mov_b32 m0, %0\n\ts_nop 4\n\tbuffer_load_dwordx4 %1, %2, %3 offen nt lds" ::"s"(lds_dst), "v"(voff), "s"(srd), "s"(soff) : "memory");
}
DI void dma_c(unsigned lds_dst, unsigned voff, rsrc_t srd, unsigned soff) {
  asm volatile("s_mov_b32 m0, %0\n\ts_nop 4\n\tbuffer_load_dwordx4 %1, %2, %3 offen lds" ::"s"(lds_dst), "v"(voff), "s"(srd), "s"(soff) : "memory");
}
DI v4u ld16(rsrc_t rs, int off) { return __builtin_amdgcn_raw_buffer_load_b128(rs, off, 0, AUX_SC1); }
DI v2u ld8(rsrc_t rs, int off) { return __builtin_amdgcn_raw_buffer_load_b64(rs, off, 0, AUX_SC1); }
DI unsigned ld4(rsrc_t rs, int off) { return __builtin_amdgcn_raw_buffer_load_b32(rs, off, 0, AUX_SC1); }
DI void st16(rsrc_t rs, int off, v4u v) { __builtin_amdgcn_raw_buffer_store_b128(v, rs, off, 0, AUX_SC1); }
DI void st8(rsrc_t rs, int off, v2u v) { __builtin_amdgcn_raw_buffer_store_b64(v, rs, off, 0, AUX_SC1); }
DI void st4(rsrc_t rs, int off, unsigned v) { __builtin_amdgcn_raw_buffer_store_b32(v, rs, off, 0, AUX_SC1); }
DI float bflo(unsigned u) { return __uint_as_float(u << 16); }
DI float bfhi(unsigned u) { return __uint_as_float(u & 0xffff0000u); }
DI unsigned pk2(float a, float b) { f32x2 v = {a, b}; return __builtin_bit_cast(unsigned, __builtin_convertvector(v, bf16x2)); }
DI unsigned unsent(unsigned u) { return u < 0xFFFFFFFEu ? u : 0xFFFFFFFEu; }
DI unsigned umax4(v4u v) { return max(max(v.x, v.y), max(v.z, v.w)); }
DI unsigned swz(unsigned row) { return ((row & 3u) << 2) | ((row >> 2) & 3u); }
DI f32x4 mfma16(v4u a, v4u b, f32x4 c) { return __builtin_amdgcn_mfma_f32_16x16x32_bf16(__builtin_bit_cast(bf16x8, a), __builtin_bit_cast(bf16x8, b), c, 0, 0, 0); }
DI f32x16 mfma32(v4u a, v4u b, f32x16 c) { return __builtin_amdgcn_mfma_f32_32x32x16_bf16(__builtin_bit_cast(bf16x8, a), __builtin_bit_cast(bf16x8, b), c, 0, 0, 0); }
DI v2u trd(LAS unsigned char* L, unsigned a) { return __builtin_bit_cast(v2u, __builtin_amdgcn_ds_read_tr16_b64_v4i16((LAS s16x4*)(L + a))); }
DI v4u trd2(LAS unsigned char* L, unsigned a0, unsigned a1) { v2u x = trd(L, a0), y = trd(L, a1); return v4u{x.x, x.y, y.x, y.y}; }
DI v4u lds16(LAS unsigned char* L, unsigned a) { return *(LAS v4u*)(L + a); }
DI float mx(float a, float b) { return __builtin_elementwise_maximum(a, b); }
DI float x16(float v) { auto r = __builtin_amdgcn_permlane16_swap(__float_as_uint(v), __float_as_uint(v), false, false); return __uint_as_float(r[0]) + __uint_as_float(r[1]); }
DI float x32(float v) { auto r = __builtin_amdgcn_permlane32_swap(__float_as_uint(v), __float_as_uint(v), false, false); return __uint_as_float(r[0]) + __uint_as_float(r[1]); }
DI float m32(float v) { auto r = __builtin_amdgcn_permlane32_swap(__float_as_uint(v), __float_as_uint(v), false, false); return mx(__uint_as_float(r[0]), __uint_as_float(r[1])); }
template <int CTRL> DI float dppf(float v) { return __uint_as_float(__builtin_amdgcn_update_dpp(0u, __float_as_uint(v), CTRL, 0xf, 0xf, true)); }
DI unsigned dppu(unsigned v) { return __builtin_amdgcn_update_dpp(0u, v, 0xB1, 0xf, 0xf, true); }
DI float rsum(float v) { v += dppf<0xB1>(v); v += dppf<0x4E>(v); v += dppf<0x141>(v); v += dppf<0x140>(v); return v; }
DI float hsum(float v) { return x16(rsum(v)); }
DI float wsum(float v) { return x32(hsum(v)); }
DI void lds_arrive(LAS unsigned char* L, unsigned a) { if ((threadIdx.x & 63) == 0) __hip_atomic_fetch_add((LAS unsigned*)(L + a), 1u, __ATOMIC_RELAXED, __HIP_MEMORY_SCOPE_WORKGROUP); }
DI void lds_await(LAS unsigned char* L, unsigned a, unsigned n) {
  for (int sp = 0; sp < (1 << 22); sp++) { if (__builtin_amdgcn_readfirstlane(*(volatile LAS unsigned*)(L + a)) >= n) break; }
  CFENCE();
}
DI void wait_until(unsigned long long t0, unsigned ticks) { while (__builtin_amdgcn_s_memrealtime() - t0 < ticks) __builtin_amdgcn_s_sleep(1); }

constexpr unsigned XL_WUV = 0, XL_FW = 32768, XL_WQ = 114688, XL_MISC = 155648;
constexpr unsigned XL_XB = XL_MISC;
constexpr unsigned XL_TAG = XL_MISC + 1536;
constexpr unsigned XL_CNT = XL_MISC + 1600;
constexpr unsigned XL_QB = XL_MISC + 2048;
constexpr unsigned XL_ML = XL_MISC + 4608;
constexpr unsigned XL_PC = XL_WQ;
constexpr unsigned XL_HB = XL_WQ + 8192;
constexpr unsigned XL_STAT = XL_WQ + 16384;
constexpr unsigned XL_RED = XL_WQ + 17408;
constexpr unsigned YL_C = 0, YL_Q = 131072;
constexpr unsigned YL_P = YL_Q;
constexpr unsigned YL_SMAX = YL_Q + 8192;
constexpr unsigned YL_SSUM = YL_Q + 10240;
constexpr unsigned YL_W = 0;
constexpr unsigned YL_RED = YL_Q + 12288;
constexpr unsigned YL_ACT = YL_Q + 20480;
constexpr unsigned L_OOB = 0x30000;
constexpr int G_OOB = 0x40000000;

__global__ void __launch_bounds__(512, 2)
mla_block_kernel(const u16* x, const u16* ckv, const u16* wq, const u16* wuk, const u16* wuv, const u16* wo, const u16* w1, const u16* w3,
                 const u16* w2, u16* out, unsigned char* ws) {
  __shared__ __attribute__((aligned(16))) unsigned char smem[163840];
  LAS unsigned char* L = (LAS unsigned char*)smem;
  const unsigned lbase = (unsigned)(size_t)L;
  const int tid = threadIdx.x, lane = tid & 63, w = __builtin_amdgcn_readfirstlane(tid >> 6);
  const int bid = blockIdx.x;
  const int l15 = lane & 15, g = lane >> 4;
  if (w == 5 && (bid >> 3) < CODE_KB) {
    unsigned long long pc0;
    asm volatile("s_getpc_b64 %0" : "=s"(pc0));
    const unsigned co = (bid >> 3) * 1024 + lane * 16;
    asm volatile("s_mov_b32 m0, %0\n\ts_nop 0\n\tglobal_load_lds_dwordx4 %1, %2" ::"s"(lbase + (bid < 128 ? 65536u : 139264u)), "v"(co), "s"(pc0 & ~255ull) : "memory");
  }
  const unsigned long long t_start = __builtin_amdgcn_s_memrealtime();
  STAMP_DECL
  STAMP(0);
  if (tid < 3) *(LAS v4u*)(L + XL_CNT + tid * 16) = v4u{0u, 0u, 0u, 0u};
  asm volatile("" : "+s"(x), "+s"(ckv), "+s"(wq), "+s"(wuk), "+s"(wuv), "+s"(wo), "+s"(w1), "+s"(w3), "+s"(w2), "+s"(out), "+s"(ws));
  BAR();
  const rsrc_t rw = mk_rsrc(ws, WS_BYTES);
  const int h = (bid & 7) + 8 * ((bid >> 5) & 3), r = (bid >> 3) & 3;
  const unsigned vb = g * 8192 + ((l15 ^ (g << 2)) << 4);
  unsigned ep = 0, xr[2][5];
  if (bid < 128 && w < 4) {
    const rsrc_t rs = mk_rsrc(wq, M_ * 8192);
    unsigned ld = lbase + XL_WQ + w * 10240, so = (160 * r + 40 * w) * 8192 + h * 256;
#pragma unroll 1
    for (int i = 0; i < 10; i++) { dma(ld, vb ^ (((10 * w + i) & 3) << 4), rs, so); ld += 1024; so += 32768; }
    if (w == 0) dma_c(lbase + XL_XB, (lane & 31) < 20 ? (lane >> 5) * 1280 + (lane & 31) * 16 : G_OOB, mk_rsrc(x, 2 * M_ * 2), r * 320);
    STAMP(1);
  } else {
    ep = ld4(rw, OFF_EPOCH);
    if (bid < 128) {
      if (w == 4) {
        const v4u sv = {SENT, SENT, SENT, SENT};
        st16(rw, lane < 32 ? OFF_QA + ((lane >> 4) * 32 + h) * 1024 + r * 256 + l15 * 16 : OFF_HP + bid * 512 + (lane - 32) * 16, sv);
        st16(rw, OFF_CX + (h * 4 + r) * 1024 + lane * 16, sv);
      } else if (w == 5) {
#pragma unroll
        for (int bb = 0; bb < 2; bb++)
#pragma unroll
          for (int k = 0; k < 5; k++) xr[bb][k] = ((const GAS unsigned*)x)[bb * 320 + k * 64 + lane];
      }
    }
    CFENCE();
  }
  const int j = bid & 127;
  const int q4 = l15 >> 2, p4 = lane & 3;

  if (bid < 128) {
    const unsigned tch = 2 * w + (p4 >> 1), tr0 = 8 * g + q4, tr1 = tr0 + 4;
    const unsigned ta0 = tr0 * 256 + ((tch ^ swz(tr0)) << 4) + 8 * (p4 & 1), ta1 = tr1 * 256 + ((tch ^ swz(tr1)) << 4) + 8 * (p4 & 1);
    unsigned tag;
    if (w < 4) {
      unsigned aa = XL_XB + ((lane & 2) ? 2 : (lane & 1)) * 512 + g * 16, b0 = XL_WQ + ta0, b1 = XL_WQ + ta1, b2 = b0 ^ 128, b3 = b1 ^ 128;
      int so1 = lane < 32 ? OFF_Q1 + ((h * 4 + r) * 128 + 16 * w + 64 * g + l15) * 16 : G_OOB;
      PIN(aa); PIN(b0); PIN(b1); PIN(b2); PIN(b3); PIN(so1);
      VM_WAIT(0);
      STAMP(14);
      *(volatile LAS u16*)(L + XL_CNT + 16 + w * 2) = 1;
      for (int sp = 0; sp < (1 << 22); sp++) {
        const v4u f = *(volatile LAS v4u*)(L + XL_CNT + 16);
        if (__builtin_amdgcn_readfirstlane(f.x & f.y & f.z) == 0x00010001u) break;
      }
      CFENCE();
      STAMP(2);
      v4u a[5], bq[2][5];
#pragma unroll
      for (int s = 0; s < 5; s++) {
        a[s] = lds16(L, aa + s * 64);
        bq[0][s] = trd2(L, b0 + s * 8192, b1 + s * 8192);
        bq[1][s] = trd2(L, b2 + s * 8192, b3 + s * 8192);
      }
      tag = *(LAS unsigned*)(L + XL_TAG);
      const f32x4 st = *(LAS f32x4*)(L + XL_TAG + 16);
      SB();
      f32x4 acc[2] = {{0.f, 0.f, 0.f, 0.f}, {0.f, 0.f, 0.f, 0.f}};
#pragma unroll
      for (int s = 0; s < 5; s++)
#pragma unroll
        for (int t = 0; t < 2; t++) acc[t] = mfma16(a[s], bq[t][s], acc[t]);
      const f32x4 ac = g ? acc[1] : acc[0];
      st16(rw, so1, v4u{__float_as_uint((ac[0] - st[0] * ac[2]) * st[1]), tag, __float_as_uint((ac[1] - st[2] * ac[2]) * st[3]), tag});
      STAMP(3);
    } else if (w == 4) {
      *(LAS v2u*)(L + XL_XB + 1024 + lane * 8) = v2u{0x3F803F80u, 0x3F803F80u};
      tag = ep * 1664525u + 1013904223u;
      if (lane == 0) *(LAS unsigned*)(L + XL_TAG) = tag;
      CFENCE();
      *(volatile LAS u16*)(L + XL_CNT + 16 + w * 2) = 1;
      VM_WAIT(0);
      if (lane == 0) st8(rw, OFF_RDY + bid * 128, v2u{tag, ~tag});
      lds_arrive(L, XL_CNT + 4);
      STAMP(1);
    } else if (w == 5) {
      const unsigned pre = ld4(rw, OFF_Q1 + h * 8192 + lane * 128);
#pragma unroll
      for (int bb = 0; bb < 2; bb++) {
        float s = 0.f;
#pragma unroll
        for (int k = 0; k < 5; k++) s += bflo(xr[bb][k]) + bfhi(xr[bb][k]);
        const float mean = wsum(s) * (1.f / M_);
        float q = 0.f;
#pragma unroll
        for (int k = 0; k < 5; k++) { float a = bflo(xr[bb][k]) - mean, c = bfhi(xr[bb][k]) - mean; q += a * a + c * c; }
        const float rstd = __builtin_amdgcn_rsqf(wsum(q) * (1.f / M_) + 1e-5f);
        if (lane == 0) *(LAS f32x2*)(L + XL_TAG + 16 + bb * 8) = f32x2{mean, rstd};
      }
      CFENCE();
      *(volatile LAS u16*)(L + XL_CNT + 16 + w * 2) = 1;
      asm volatile("" ::"v"(pre));
      STAMP(1);
      tag = ep * 1664525u + 1013904223u;
    } else {
      const int u = w - 6;
      if (UK_DELAY) wait_until(t_start, UK_DELAY);
      {
        const rsrc_t rk = mk_rsrc(wuk, 512 * 8192);
        unsigned ld = lbase + XL_WUV + u * 16384, so = (128 * r + 64 * u) * 8192 + h * 256;
#pragma unroll 1
        for (int i = 0; i < 16; i++) { dma(ld, vb ^ ((i & 3) << 4), rk, so); ld += 1024; so += 32768; }
      }
      STAMP(1);
      int qo = OFF_Q1 + (h * 512 + lane) * 16;
      unsigned qw = XL_QB + u * 1088 + lane * 2, qr = XL_QB + u * 1088 + (lane & 3) * 272 + g * 16;
      int so2 = (lane & 1) ? G_OOB : OFF_QA + h * 1024 + r * 256 + u * 128 + lane * 2;
      PIN(qo); PIN(qw); PIN(qr); PIN(so2);
      VM_WAIT(0);
      tag = ep * 1664525u + 1013904223u;
      lds_await(L, XL_CNT + 4, 1);
      v4u bk[4][4];
#pragma unroll
      for (int t = 0; t < 4; t++)
#pragma unroll
        for (int s = 0; s < 4; s++) bk[t][s] = lds16(L, XL_WUV + u * 16384 + t * 4096 + l15 * 256 + (((4 * s + g) ^ swz(l15)) << 4));
      STAMP(3);
      v4u v[2][4];
      SPIN {
        CFENCE();
        bool ok = true;
#pragma unroll
        for (int e = 0; e < 2; e++)
#pragma unroll
          for (int k = 0; k < 4; k++) v[e][k] = ld16(rw, qo + k * 2048 + e * 1024);
#pragma unroll
        for (int e = 0; e < 2; e++)
#pragma unroll
          for (int k = 0; k < 4; k++) ok &= (v[e][k].y == tag) & (v[e][k].w == tag);
        if (__all(ok)) break;
      }
      STAMP(4);
#pragma unroll
      for (int e = 0; e < 2; e++) {
        const float q0 = (__uint_as_float(v[e][0].x) + __uint_as_float(v[e][1].x)) + (__uint_as_float(v[e][2].x) + __uint_as_float(v[e][3].x));
        const float q1 = (__uint_as_float(v[e][0].z) + __uint_as_float(v[e][1].z)) + (__uint_as_float(v[e][2].z) + __uint_as_float(v[e][3].z));
        const unsigned hi = pk2(q0, q1), lo = pk2(q0 - bflo(hi), q1 - bfhi(hi));
        *(LAS u16*)(L + qw + e * 128) = (u16)hi;
        *(LAS u16*)(L + qw + e * 128 + 272) = (u16)(hi >> 16);
        *(LAS u16*)(L + qw + e * 128 + 544) = (u16)lo;
        *(LAS u16*)(L + qw + e * 128 + 816) = (u16)(lo >> 16);
      }
      CFENCE();
      v4u a[4];
#pragma unroll
      for (int s = 0; s < 4; s++) a[s] = lds16(L, qr + s * 64);
      f32x4 acc[4];
#pragma unroll
      for (int t = 0; t < 4; t++) acc[t] = f32x4{0.f, 0.f, 0.f, 0.f};
#pragma unroll
      for (int s = 0; s < 4; s++)
#pragma unroll
        for (int t = 0; t < 4; t++) acc[t] = mfma16(a[s], bk[t][s], acc[t]);
      const f32x4 ac = (g & 2) ? ((g & 1) ? acc[3] : acc[2]) : ((g & 1) ? acc[1] : acc[0]);
      const float sc = 0.08838834764831845f * 1.4426950408889634f;
      const unsigned me = pk2((ac[0] + ac[2]) * sc, (ac[1] + ac[3]) * sc), nb = dppu(me);
      st4(rw, so2, unsent(__builtin_amdgcn_perm(nb, me, 0x05040100u)));
      st4(rw, so2 + 32768, unsent(__builtin_amdgcn_perm(nb, me, 0x07060302u)));
      STAMP(5);
    }
    const bool isF1 = bid < 54;
    const int mat = bid & 1, fc = bid >> 1;
    BAR();
    const rsrc_t r1 = mk_rsrc(mat ? w3 : w1, M_ * F_ * 2);
    {
      if (X_LATE_DELAY) wait_until(t_start, X_LATE_DELAY);
      const rsrc_t rv = mk_rsrc(wuv, 512 * 8192);
#pragma unroll 1
      for (int i = w; i < 32; i += 8) { VM_WIN(X_WIN); dma(lbase + XL_WUV + i * 1024, vb ^ ((i & 3) << 4), rv, (128 * r + 4 * i) * 8192 + h * 256); }
      if (isF1) {
#pragma unroll 1
        for (int i = w; i < 40; i += 8) {
          VM_WIN(X_WIN);
          dma(lbase + XL_FW + i * 1024, (lane >> 3) * 3456 + (lane & 7) * 16, r1, (i * 8) * 3456 + fc * 128);
        }
      }
    }
    const int bw = w >> 2, cg = w & 3;
    const int hwv = 2 * w + (lane >> 5), fb = hwv >= 5 ? 1 : 0, fd = (hwv - 5 * fb) * 64 + (lane & 31);
    unsigned xv[2] = {0u, 0u};
    if (w < 5) { xv[0] = ((const GAS unsigned*)x)[fb * 320 + fd]; xv[1] = ((const GAS unsigned*)x)[fb * 320 + fd + 32]; }
    VM_WAIT(0);
    BAR();
    STAMP(6);
    v4u fv[2][4];
    const int ft = w & 3, kh = w >> 2;
    {
      const unsigned vch = 4 * cg + (p4 >> 1);
      const unsigned va0 = XL_WUV + tr0 * 256 + ((vch ^ swz(tr0)) << 4) + 8 * (p4 & 1), va1 = XL_WUV + tr1 * 256 + ((vch ^ swz(tr1)) << 4) + 8 * (p4 & 1);
#pragma unroll
      for (int t = 0; t < 2; t++)
#pragma unroll
        for (int s = 0; s < 4; s++) fv[t][s] = trd2(L, (va0 ^ (t << 5)) + s * 8192, (va1 ^ (t << 5)) + s * 8192);
      if (tid < 320) *(LAS unsigned*)(L + XL_HB + 4 * 1296 + tid * 4) = 0x3F803F80u;
    }
    {
      int sto = OFF_ST + ((bw * 64 + 16 * cg + l15) * 32 + h) * 16, oo = OFF_O + (((h * 4 + r) * 2 + bw) * 64 + 16 * cg) * 256 + lane * 16;
      unsigned pcw = XL_PC + (4 * w + g) * 256 + ((l15 ^ (4 * cg + g)) << 4), pcr = XL_PC + (16 * bw + l15) * 256 + ((g ^ l15) << 4);
      int cxo = (lane & 1) ? G_OOB : OFF_CX + ((h * 4 + r) * 8 + w) * 128 + (g & 1) * 64 + (g >> 1) * 32 + l15 * 2;
      int shf = g * 4; unsigned mla = XL_ML + bw * 32;
      PIN(sto); PIN(oo); PIN(pcw); PIN(pcr); PIN(cxo); PIN(shf); PIN(mla);
      v4u sv = {0u, 0u, 0u, 0u};
      if (lane < 16) {
        SPIN {
          CFENCE();
          sv = ld16(rw, sto);
          if (__all((sv.y == tag) & (sv.w == tag))) break;
          if (SLP_ST) __builtin_amdgcn_s_sleep(SLP_ST);
        }
      }
      STAMP(7);
      v4u pc[4];
#pragma unroll
      for (int i = 0; i < 4; i++) pc[i] = ld16(rw, oo + i * 1024);
      SB();
      const float mj = __uint_as_float(sv.x), lj = __uint_as_float(sv.z);
      float mm = mj;
      mm = mx(mm, dppf<0xB1>(mm)); mm = mx(mm, dppf<0x4E>(mm)); mm = mx(mm, dppf<0x141>(mm)); mm = mx(mm, dppf<0x140>(mm));
      const float ls = rsum(__builtin_amdgcn_exp2f(mj - mm) * lj);
      if (lane == 0) *(LAS f32x2*)(L + XL_ML + w * 8) = f32x2{mm, ls};
      BAR();
      const f32x4 m01 = *(LAS f32x4*)(L + mla), m23 = *(LAS f32x4*)(L + mla + 16);
      const float Mx = mx(mx(m01[0], m01[2]), mx(m23[0], m23[2]));
      const float Ls = (__builtin_amdgcn_exp2f(m01[0] - Mx) * m01[1] + __builtin_amdgcn_exp2f(m01[2] - Mx) * m01[3]) + (__builtin_amdgcn_exp2f(m23[0] - Mx) * m23[1] + __builtin_amdgcn_exp2f(m23[2] - Mx) * m23[3]);
      const float wgt = __builtin_amdgcn_exp2f(mj - Mx) * __builtin_amdgcn_rcpf(Ls);
      float wi[4];
#pragma unroll
      for (int i = 0; i < 4; i++) wi[i] = __uint_as_float(__builtin_amdgcn_ds_bpermute(shf + 16 * i, __float_as_uint(wgt)));
      SB();
      f32x2 a[4];
#pragma unroll
      for (int i = 0; i < 4; i++) {
#pragma unroll
        for (int k = 0; k < 4; k++) {
          const f32x2 v = {bflo(pc[i][k]), bfhi(pc[i][k])};
          if (i == 0) a[k] = v * wi[0]; else a[k] += v * wi[i];
        }
      }
      *(LAS v4u*)(L + pcw) = v4u{pk2(a[0][0], a[0][1]), pk2(a[1][0], a[1][1]), pk2(a[2][0], a[2][1]), pk2(a[3][0], a[3][1])};
      STAMP(8);
      BAR();
      v4u pa[4];
#pragma unroll
      for (int s = 0; s < 4; s++) pa[s] = lds16(L, pcr ^ (s << 6));
      SB();
      f32x4 acc[2] = {{0.f, 0.f, 0.f, 0.f}, {0.f, 0.f, 0.f, 0.f}};
#pragma unroll
      for (int s = 0; s < 4; s++)
#pragma unroll
        for (int t = 0; t < 2; t++) acc[t] = mfma16(pa[s], fv[t][s], acc[t]);
      const float s0 = (acc[0][0] + acc[0][1]) + (acc[0][2] + acc[0][3]), s1 = (acc[1][0] + acc[1][1]) + (acc[1][2] + acc[1][3]);
      auto sw = __builtin_amdgcn_permlane32_swap(__float_as_uint(s0), __float_as_uint(s1), false, false);
      const float cx = x16(__uint_as_float(sw[0]) + __uint_as_float(sw[1]));
      const unsigned hi = pk2(cx, cx);
      const unsigned me = (g & 1) ? pk2(cx - bflo(hi), 0.f) : hi;
      st4(rw, cxo, unsent(__builtin_amdgcn_perm(dppu(me), me, 0x05040100u)));
      STAMP(9);
    }
    if (isF1) {
#pragma unroll 1
      for (int i = 40 + w; i < 80; i += 8) dma(lbase + XL_FW + i * 1024, (lane >> 3) * 3456 + (lane & 7) * 16, r1, (i * 8) * 3456 + fc * 128);
      VM_WAIT(0);
      lds_arrive(L, XL_CNT + 4);
      if (w >= 4) lds_await(L, XL_CNT + 4, 9);
      STAMP(12);
      v4u fw[10];
      {
        const unsigned b0 = XL_FW + (320 * kh + 8 * g + q4) * 128 + ft * 32 + 8 * p4;
#pragma unroll
        for (int s = 0; s < 10; s++) fw[s] = trd2(L, b0 + s * 4096, b0 + s * 4096 + 512);
      }
      int ho = OFF_HP + fb * 1280 + fd * 4;
      unsigned hw = XL_HB + fb * 1296 + fd * 4, ar = XL_HB + (l15 < 4 ? l15 : 4) * 1296 + kh * 640 + g * 16;
      unsigned rdw = lane < 32 ? XL_RED + (w * 16 + l15) * 16 + g * 8 : L_OOB;
      PIN(ho); PIN(hw); PIN(ar); PIN(rdw);
      f32x4 xs = f32x4{bflo(xv[0]), bfhi(xv[0]), bflo(xv[1]), bfhi(xv[1])} - *(LAS float*)(L + XL_TAG + 16 + fb * 8);
      unsigned stw = (lane & 31) == 0 ? XL_STAT + (hwv + fb) * 8 : L_OOB, str = XL_STAT + (lane >> 5) * 48, rdr = XL_RED + (32 * (w & 1) + (lane & 31)) * 16;
      int aco = (lane & 1) ? G_OOB : OFF_ACT + (mat * 54 + 2 * fc + (w & 1)) * 128 + lane * 2;
      PIN(xs); PIN(stw); PIN(str); PIN(rdr); PIN(aco);
      f32x4 hv = {0.f, 0.f, 0.f, 0.f};
      if (w < 5) {
        unsigned hp[8][2];
        SPIN {
          CFENCE();
          unsigned m = 0;
#pragma unroll
          for (int k = 0; k < 8; k++) { hp[k][0] = ld4(rw, ho + k * 2560); hp[k][1] = ld4(rw, ho + k * 2560 + 128); }
#pragma unroll
          for (int k = 0; k < 8; k++) m = max(m, max(hp[k][0], hp[k][1]));
          if (__all(m != SENT)) break;
          if (SLP_O) __builtin_amdgcn_s_sleep(SLP_O);
        }
        STAMP(10);
        hv = xs;
        f32x4 hv2 = {0.f, 0.f, 0.f, 0.f};
#pragma unroll
        for (int k = 0; k < 8; k += 2) {
          hv += f32x4{bflo(hp[k][0]), bfhi(hp[k][0]), bflo(hp[k][1]), bfhi(hp[k][1])};
          hv2 += f32x4{bflo(hp[k + 1][0]), bfhi(hp[k + 1][0]), bflo(hp[k + 1][1]), bfhi(hp[k + 1][1])};
        }
        hv += hv2;
        const unsigned h0 = pk2(hv[0], hv[1]), h1 = pk2(hv[2], hv[3]);
        *(LAS unsigned*)(L + hw) = h0;
        *(LAS unsigned*)(L + hw + 128) = h1;
        *(LAS unsigned*)(L + hw + 2592) = pk2(hv[0] - bflo(h0), hv[1] - bfhi(h0));
        *(LAS unsigned*)(L + hw + 2592 + 128) = pk2(hv[2] - bflo(h1), hv[3] - bfhi(h1));
      }
      BAR();
      {
        v4u ha[10];
#pragma unroll
        for (int s = 0; s < 10; s++) ha[s] = lds16(L, ar + s * 64);
        SB();
        const float s1 = hsum((hv[0] + hv[1]) + (hv[2] + hv[3])), s2 = hsum((hv[0] * hv[0] + hv[1] * hv[1]) + (hv[2] * hv[2] + hv[3] * hv[3]));
        *(LAS f32x2*)(L + stw) = f32x2{s1, s2};
        f32x4 acc = {0.f, 0.f, 0.f, 0.f};
#pragma unroll
        for (int s = 0; s < 10; s++) acc = mfma16(ha[s], fw[s], acc);
        *(LAS f32x2*)(L + rdw) = f32x2{acc[0] + acc[2], acc[1] + acc[3]};
      }
      BAR();
      if (w < 2) {
        const f32x4 s0 = *(LAS f32x4*)(L + str), s1 = *(LAS f32x4*)(L + str + 16);
        const f32x2 s2 = *(LAS f32x2*)(L + str + 32);
        const f32x4 u0 = *(LAS f32x4*)(L + rdr), u1 = *(LAS f32x4*)(L + rdr + 1024);
        SB();
        const f32x4 s01 = s0 + s1, u = u0 + u1;
        const float mean = ((s01[0] + s01[2]) + s2[0]) * (1.f / M_);
        const float var = ((s01[1] + s01[3]) + s2[1]) * (1.f / M_) - mean * mean;
        const float rstd = __builtin_amdgcn_rsqf(mx(var, 0.f) + 1e-5f);
        float v = ((lane & 32 ? u[1] : u[0]) - (0.5f * mean) * u[2]) * rstd;
        if (!mat) v = v * __builtin_amdgcn_rcpf(1.0f + __builtin_amdgcn_exp2f(v * -1.4426950408889634f));
        st4(rw, aco, unsent(pk2(v, dppf<0xB1>(v))));
      }
      STAMP(11);
    }
  } else {
    const int b = j & 1, chunk = j >> 1;
    const int fj = 2 * ((j & 7) + 8 * (j >> 4)) + ((j >> 3) & 1);
    const unsigned l31 = lane & 31, hh = lane >> 5, blk = (lane >> 4) & 1;
    unsigned tag;
    v4u av[2][8];
    const unsigned pch = 8 * w + 2 * blk + (p4 >> 1), pr0 = 8 * hh + q4, pr1 = pr0 + 4;
    unsigned pa0 = YL_C + pr0 * 1024 + ((pch ^ swz(pr0)) << 4) + 8 * (p4 & 1), pa1 = YL_C + pr1 * 1024 + ((pch ^ swz(pr1)) << 4) + 8 * (p4 & 1);
    unsigned pfa = YL_P + l31 * 256 + ((hh ^ swz(l31)) << 4), sgw = YL_C + l31 * 1024 + (((8 * w) ^ swz(l31)) << 4) + 8 * hh;
    unsigned sgr = YL_C + (lane >> 3) * 1024 + (((8 * w + (lane & 7)) ^ swz(lane >> 3)) << 4);
    int oo = OFF_O + ((((lane >> 3) * 4 + (w >> 1)) * 2 + b) * 64 + chunk) * 256 + (w & 1) * 128 + (lane & 7) * 16;
    int sto = lane < 32 ? OFF_ST + ((b * 64 + chunk) * 32 + lane) * 16 : G_OOB;
    unsigned smr = YL_SMAX + (l31 >> 4) * 1024 + l15 * 16, ssr = YL_SSUM + l31 * 32;
    const bool isL2 = j < 80;
    const int kg = j / 10, nn = j % 10;
    v4u wor[8];
    if (w < 4) {
      const rsrc_t rc = mk_rsrc(ckv + ((size_t)b * SK + chunk * 128) * 512, 128 * 1024);
      if (Y_DELAY) wait_until(t_start, Y_DELAY);
#pragma unroll 1
      for (int i = 0; i < 32; i++) {
        const unsigned row = 32 * w + i;
        VM_WIN(Y_WIN);
        dma(lbase + YL_C + row * 1024, (lane ^ swz(i)) << 4, rc, row * 1024);
        if (i == A0_AT) BAR();
      }
      STAMP(1);
      const unsigned fa = l31 * 1024 + ((hh ^ swz(l31)) << 4);
      VM_WAIT(0);
      STAMP(2);
      tag = ep * 1664525u + 1013904223u;
      v4u cf[32];
#pragma unroll
      for (int s = 0; s < 32; s++) cf[s] = lds16(L, w * 32768 + (fa ^ ((s & 7) << 5)) + (s >> 3) * 256);
      unsigned fq = YL_Q + fa, smw = lane < 32 ? smr + w * 4 : L_OOB, ssw = ssr + w * 8 + hh * 4;
      unsigned pw = YL_P + l31 * 256 + (((4 * w) ^ swz(l31)) << 4) + hh * 8;
      PIN(fq); PIN(smw); PIN(smr); PIN(ssw); PIN(ssr); PIN(pw); PIN(pa0); PIN(pa1); PIN(pfa); PIN(sgw); PIN(sgr); PIN(oo); PIN(sto); PIN(tag);
      BAR();
      STAMP(4);
      f32x16 sacc, sacc2;
#pragma unroll
      for (int k = 0; k < 16; k++) { sacc[k] = 0.f; sacc2[k] = 0.f; }
      {
        v4u qf[8];
#pragma unroll
        for (int s = 0; s < 7; s++) qf[s] = lds16(L, (fq ^ ((s & 7) << 5)) + (s >> 3) * 256);
#pragma unroll
        for (int s = 0; s < 32; s++) {
          if (s + 7 < 32) qf[(s + 7) & 7] = lds16(L, (fq ^ (((s + 7) & 7) << 5)) + ((s + 7) >> 3) * 256);
          if (s >= 13 && s < 29) { const int ct = (s - 13) >> 3, ks = (s - 13) & 7; av[ct][ks] = trd2(L, (pa0 ^ (ct << 6)) + ks * 16384, (pa1 ^ (ct << 6)) + ks * 16384); }
          SB();
          if (S_ACC2 && (s & 1)) sacc2 = mfma32(cf[s], qf[s & 7], sacc2); else sacc = mfma32(cf[s], qf[s & 7], sacc);
          if (s == 10) asm volatile("s_barrier" ::: "memory");
          SB();
        }
      }
      if (S_ACC2) sacc += sacc2;
      STAMP(5);
      float mloc = mx(mx(mx(mx(sacc[0], sacc[1]), sacc[2]), mx(mx(sacc[3], sacc[4]), sacc[5])), mx(mx(mx(sacc[6], sacc[7]), sacc[8]), mx(mx(sacc[9], sacc[10]), sacc[11])));
      mloc = m32(mx(mloc, mx(mx(sacc[12], sacc[13]), mx(sacc[14], sacc[15]))));
      *(LAS float*)(L + smw) = mloc;
      STAMP(7);
      BAR();
      {
        const f32x4 mv = *(LAS f32x4*)(L + smr);
        const float m = mx(mx(mv[0], mv[1]), mx(mv[2], mv[3]));
        float p[16];
#pragma unroll
        for (int k = 0; k < 4; k++) {
#pragma unroll
          for (int e = 0; e < 4; e++) p[4 * k + e] = __builtin_amdgcn_exp2f(sacc[4 * k + e] - m);
          *(LAS v2u*)(L + (pw ^ (k << 4))) = v2u{pk2(p[4 * k], p[4 * k + 1]), pk2(p[4 * k + 2], p[4 * k + 3])};
        }
        *(LAS float*)(L + ssw) = (((p[0] + p[1]) + (p[2] + p[3])) + ((p[4] + p[5]) + (p[6] + p[7]))) + (((p[8] + p[9]) + (p[10] + p[11])) + ((p[12] + p[13]) + (p[14] + p[15])));
      }
      BAR();
      STAMP(8);
    } else {
      tag = ep * 1664525u + 1013904223u;
      wait_until(t_start, G_START);
      SPIN {
        CFENCE();
        const v2u v = ld8(rw, OFF_RDY + (32 * (w - 4) + l31) * 128);
        if (__all((v.x == tag) & (v.y == ~tag))) break;
        __builtin_amdgcn_s_sleep(16);
      }
      STAMP(1);
      int qo = OFF_QA + (b * 32 + 8 * (w - 4)) * 1024 + lane * 16;
      unsigned qw = YL_Q + 8 * (w - 4) * 1024 + ((lane ^ (2 * (w & 1))) << 4);
      PIN(qo); PIN(qw);
      BAR();
      STAMP(2);
      v4u v[8];
#pragma unroll
      for (int k = 0; k < 8; k++) v[k] = ld16(rw, qo + k * 1024);
      SPIN {
        unsigned m = 0;
#pragma unroll
        for (int k = 0; k < 8; k++) m = max(m, umax4(v[k]));
        if (__all(m != SENT)) break;
        CFENCE();
#pragma unroll
        for (int k = 0; k < 8; k++) v[k] = ld16(rw, qo + k * 1024);
      }
#pragma unroll
      for (int k = 0; k < 8; k++) *(LAS v4u*)(L + (qw ^ ((((k & 3) << 2) | (k >> 2)) << 4)) + k * 1024) = v[k];
      STAMP(3);
      BAR();
      asm volatile("s_barrier" ::: "memory");
      if (isL2) {
        const GAS v4u* op = (const GAS v4u*)(wo + (size_t)(512 * kg + 128 * (w - 4) + (lane >> 3)) * M_ + nn * 64 + (lane & 7) * 8);
#pragma unroll
        for (int i = 0; i < 8; i++) wor[i] = __builtin_nontemporal_load(op + i * 640);
      }
      SB();
#pragma unroll
      for (int ct = 0; ct < 2; ct++)
#pragma unroll
        for (int ks = 0; ks < 8; ks++) av[ct][ks] = trd2(L, (pa0 ^ (ct << 6)) + ks * 16384, (pa1 ^ (ct << 6)) + ks * 16384);
      PIN(pfa); PIN(sgw); PIN(sgr); PIN(oo);
      if (PV_PRIO) __builtin_amdgcn_s_setprio(2);
      BAR();
      BAR();
      STAMP(8);
    }
    {
      v4u pf[8];
#pragma unroll
      for (int ks = 0; ks < 8; ks++) pf[ks] = lds16(L, pfa ^ (ks << 5));
      SB();
      f32x16 o[2];
#pragma unroll
      for (int ct = 0; ct < 2; ct++) {
#pragma unroll
        for (int k = 0; k < 16; k++) o[ct][k] = 0.f;
#pragma unroll
        for (int ks = 0; ks < 8; ks++) o[ct] = mfma32(av[ct][ks], pf[ks], o[ct]);
        if (ct == 0) SB();
      }
#pragma unroll
      for (int ct = 0; ct < 2; ct++)
#pragma unroll
        for (int k = 0; k < 4; k++)
          *(LAS v2u*)(L + (sgw ^ ((4 * ct + k) << 4))) = v2u{pk2(o[ct][4 * k], o[ct][4 * k + 1]), pk2(o[ct][4 * k + 2], o[ct][4 * k + 3])};
      STAMP(9);
      CFENCE();
      v4u ov[4];
#pragma unroll
      for (int it = 0; it < 4; it++) ov[it] = lds16(L, (sgr ^ ((it & 1) << 5)) + it * 8192);
      SB();
#pragma unroll
      for (int it = 0; it < 4; it++) __builtin_amdgcn_raw_buffer_store_b128(ov[it], rw, oo, it << 20, AUX_SC1);
      STAMP(10);
      __builtin_amdgcn_s_setprio(0);
      v4u stv = {0u, 0u, 0u, 0u};
      if (w == 4) {
        const f32x4 mv = *(LAS f32x4*)(L + smr), s4 = *(LAS f32x4*)(L + ssr) + *(LAS f32x4*)(L + ssr + 16);
        stv = v4u{__float_as_uint(mx(mx(mv[0], mv[1]), mx(mv[2], mv[3]))), tag, __float_as_uint((s4[0] + s4[1]) + (s4[2] + s4[3])), tag};
      }
      PIN(stv); PIN(sto);
      VM_WAIT(0);
      BAR();
      if (w == 4) st16(rw, sto, stv);
      STAMP(11);
      if (YL_DELAY) { const unsigned long long t1 = __builtin_amdgcn_s_memrealtime(); wait_until(t1, YL_DELAY); }
    }
    if (isL2) {
      if (w >= 4) {
#pragma unroll
        for (int i = 0; i < 8; i++) *(LAS v4u*)(L + YL_W + ((w - 4) * 16 + i) * 1024 + lane * 16) = wor[i];
      } else {
        const rsrc_t ro = mk_rsrc(wo, 4096 * M_ * 2);
#pragma unroll 1
        for (int i = 0; i < 8; i++) dma(lbase + YL_W + (w * 16 + 8 + i) * 1024, (lane >> 3) * 1280 + (lane & 7) * 16, ro, (512 * kg + 128 * w + 64 + 8 * i) * 1280 + nn * 128);
        VM_WAIT(0);
      }
      BAR();
      v4u fo[4][2];
      {
        const unsigned b0 = YL_W + (64 * w + 8 * g + q4) * 128 + 8 * p4;
#pragma unroll
        for (int t = 0; t < 4; t++)
#pragma unroll
          for (int s = 0; s < 2; s++) fo[t][s] = trd2(L, b0 + s * 4096 + t * 32, b0 + s * 4096 + t * 32 + 512);
      }
      int ao = OFF_CX + (((4 * kg + (w >> 1)) * 8 + (l15 >> 1)) * 4 + 2 * (w & 1)) * 128 + (l15 & 1) * 64 + g * 16;
      unsigned rdw = YL_RED + ((g >> 1) * 64 + 32 * (g & 1) + l15) * 48 + w * 4, rdr = YL_RED + (tid & 127) * 48;
      int hpo = (lane & 1) ? G_OOB : OFF_HP + ((kg * 2 + (w & 1)) * 640 + 64 * nn + lane) * 2;
      PIN(ao); PIN(rdw); PIN(rdr); PIN(hpo);
      STAMP(12);
      v4u a[2];
      SPIN {
        CFENCE();
#pragma unroll
        for (int s = 0; s < 2; s++) a[s] = ld16(rw, ao + s * 128);
        if (__all(max(umax4(a[0]), umax4(a[1])) != SENT)) break;
        if (SLP_O) __builtin_amdgcn_s_sleep(SLP_O);
      }
      STAMP(13);
      f32x4 acc[4];
#pragma unroll
      for (int t = 0; t < 4; t++) acc[t] = f32x4{0.f, 0.f, 0.f, 0.f};
#pragma unroll
      for (int s = 0; s < 2; s++)
#pragma unroll
        for (int t = 0; t < 4; t++) acc[t] = mfma16(a[s], fo[t][s], acc[t]);
      float c2[4];
#pragma unroll
      for (int t = 0; t < 4; t++) {
        auto sw = __builtin_amdgcn_permlane32_swap(__float_as_uint(acc[t][0] + acc[t][1]), __float_as_uint(acc[t][2] + acc[t][3]), false, false);
        c2[t] = __uint_as_float(sw[0]) + __uint_as_float(sw[1]);
      }
#pragma unroll
      for (int t = 0; t < 2; t++) {
        auto sw = __builtin_amdgcn_permlane16_swap(__float_as_uint(c2[t]), __float_as_uint(c2[t + 2]), false, false);
        *(LAS float*)(L + rdw + t * 768) = __uint_as_float(sw[0]) + __uint_as_float(sw[1]);
      }
      BAR();
      if (w < 2) {
        const f32x4 s4 = *(LAS f32x4*)(L + rdr) + *(LAS f32x4*)(L + rdr + 16);
        const float s = (s4[0] + s4[1]) + (s4[2] + s4[3]);
        st4(rw, hpo, unsent(pk2(s, dppf<0xB1>(s))));
      }
      STAMP(14);
    } else if (fj >= 108) {
      const int n = fj - 108;
      {
        const rsrc_t r2 = mk_rsrc(w2, F_ * M_ * 2);
#pragma unroll 1
        for (int i = 0; i < 14; i++) {
          VM_WIN(F_WIN);
          dma(lbase + YL_W + (w * 14 + i) * 1024, (lane >> 2) * 1280 + (lane & 3) * 16, r2, (w * 14 + i) * 16 * 1280 + n * 64);
        }
      }
      float hres = bflo(((const GAS u16*)x)[(lane >> 5) * M_ + 32 * n + (lane & 31)]);
      VM_WAIT(0);
      v4u fw[2][7];
      {
        const unsigned b0 = YL_W + (224 * w + 8 * g + q4) * 64 + 8 * p4;
#pragma unroll
        for (int t = 0; t < 2; t++)
#pragma unroll
          for (int s = 0; s < 7; s++) fw[t][s] = trd2(L, b0 + s * 2048 + t * 32, b0 + s * 2048 + t * 32 + 256);
      }
      STAMP(12);
      if (w == 0) {
        unsigned hp[8];
        SPIN {
          CFENCE();
          unsigned m = 0;
#pragma unroll
          for (int k = 0; k < 8; k++) hp[k] = ld4(rw, OFF_HP + ((k * 2 + (lane >> 5)) * 640 + 32 * n + (lane & 30)) * 2);
#pragma unroll
          for (int k = 0; k < 8; k++) m = max(m, hp[k]);
          if (__all(m != SENT)) break;
        }
        float he = hres, ho = 0.f;
#pragma unroll
        for (int k = 0; k < 8; k += 2) { he += (lane & 1) ? bfhi(hp[k]) : bflo(hp[k]); ho += (lane & 1) ? bfhi(hp[k + 1]) : bflo(hp[k + 1]); }
        hres = he + ho;
      }
      int ao = OFF_ACT + min(7 * w * 128 + lane * 16, 54 * 128 - 16);
      unsigned rdw = lane < 32 ? YL_RED + (16 * g + l15) * 48 + w * 4 : L_OOB, rdr = YL_RED + lane * 48;
      unsigned aw = YL_ACT + w * 1024 + lane * 16, ard = YL_ACT + w * 1024 + (lane & 1) * 64 + g * 16;
      int outo = (lane & 1) ? G_OOB : ((lane >> 5) * M_ + 32 * n + (lane & 31)) * 2;
      PIN(ao); PIN(rdw); PIN(rdr); PIN(aw); PIN(ard); PIN(outo);
      v4u sg = {0u, 0u, 0u, 0u}, up = {0u, 0u, 0u, 0u};
      if (lane < 56) {
        SPIN {
          CFENCE();
          sg = ld16(rw, ao); up = ld16(rw, ao + 6912);
          if (__all(max(umax4(sg), umax4(up)) != SENT)) break;
          if (SLP_O) __builtin_amdgcn_s_sleep(SLP_O);
        }
      }
      STAMP(13);
      {
        v4u pr;
#pragma unroll
        for (int k = 0; k < 4; k++) pr[k] = pk2(bflo(sg[k]) * bflo(up[k]), bfhi(sg[k]) * bfhi(up[k]));
        *(LAS v4u*)(L + aw) = pr;
      }
      CFENCE();
      v4u a[7];
#pragma unroll
      for (int s = 0; s < 7; s++) a[s] = lds16(L, ard + s * 128);
      f32x4 acc[2] = {{0.f, 0.f, 0.f, 0.f}, {0.f, 0.f, 0.f, 0.f}};
#pragma unroll
      for (int s = 0; s < 7; s++)
#pragma unroll
        for (int t = 0; t < 2; t++) acc[t] = mfma16(a[s], fw[t][s], acc[t]);
      {
        const f32x4 ac = g ? acc[1] : acc[0];
        *(LAS float*)(L + rdw) = ac[0];
        *(LAS float*)(L + rdw + 32 * 48) = ac[1];
      }
      BAR();
      if (w == 0) {
        const f32x4 s4 = *(LAS f32x4*)(L + rdr) + *(LAS f32x4*)(L + rdr + 16);
        const float s = hres + ((s4[0] + s4[1]) + (s4[2] + s4[3]));
        __builtin_amdgcn_raw_buffer_store_b32(pk2(s, dppf<0xB1>(s)), mk_rsrc(out, 2 * M_ * 2), outo, 0, 0);
      }
      if (n == 0 && w == 4 && lane == 0) st4(rw, OFF_EPOCH, tag);
      STAMP(14);
    }
  }
  STAMP_FLUSH;
}

extern "C" void kernel_launch(void* const* d_in, const int* in_sizes, int n_in, void* d_out, int out_size, void* d_ws, size_t ws_size,
                              hipStream_t stream) {
  mla_block_kernel<<<256, 512, 0, stream>>>((const u16*)d_in[0], (const u16*)d_in[1], (const u16*)d_in[2], (const u16*)d_in[3], (const u16*)d_in[4],
                                            (const u16*)d_in[5], (const u16*)d_in[7], (const u16*)d_in[8], (const u16*)d_in[9], (u16*)d_out,
                                            (unsigned char*)d_ws);
}
```
